```python
import torch

def _softmax(x, axis=-1):
    return torch.softmax(x, dim=axis)
def _gelu(x):
    return torch.nn.functional.gelu(x, approximate="tanh")
def _silu(x):
    return torch.nn.functional.silu(x)
def _sigmoid(x):
    return torch.sigmoid(x)
def _layernorm(x, axis=-1, eps=1e-5):
    m = x.mean(dim=axis, keepdim=True)
    v = x.var(dim=axis, keepdim=True, unbiased=False)
    return (x - m) / torch.sqrt(v + eps)
def _scatter_add(tgt, idx, src, axis=0):
    return tgt.clone().index_add_(axis, idx, src)
def _segment_sum(src, seg, n):
    out = torch.zeros((n,) + src.shape[1:], dtype=src.dtype)
    return out.index_add_(0, seg, src)
def _pad(x, pw):
    flat = tuple(v for pair in reversed(pw) for v in pair)
    return torch.nn.functional.pad(x, flat)

kernel_name = 'parallel_sum__ssm_conv_s4d__dense_activation_relu__prompt'

def setup_inputs():

    _w = (lambda a: (lambda t: t.to(torch.bfloat16) if t.is_floating_point() else t)(torch.as_tensor(a)))
    def _A_setup():
        Bsz, L, D, N = 1, 2048, 512, 128
        return {
            "u": _w((lambda *s: torch.randn(*s, dtype=torch.bfloat16) * 0.05)(Bsz, L, D)),
            "log_A_real": _w((lambda *s: torch.randn(*s, dtype=torch.bfloat16) * 3.2)(D, N) * 0.5),
            "A_imag": _w((lambda *s: torch.randn(*s, dtype=torch.bfloat16) * 0.1)(D, N) * 3.14),
            "C_real": _w((lambda *s: torch.randn(*s, dtype=torch.bfloat16) * 1.6)(D, N) / N ** 0.5),
            "C_imag": _w((lambda *s: torch.randn(*s, dtype=torch.bfloat16) * 1.6)(D, N) / N ** 0.5),
            "log_dt": _w((lambda *s: torch.randn(*s, dtype=torch.bfloat16) * 0.1)(D) * 0.5 - 4.0),
            "D_skip": _w((lambda *s: torch.randn(*s, dtype=torch.bfloat16) * 0.2)(D)),
        }
    _A_in = _A_setup()

    def _B_setup():
        B, S, D, Dff = 1, 2048, 512, 1792
        return {
            "x": _w((lambda *s: torch.randn(*s, dtype=torch.bfloat16) * 0.05)(B, S, D)),
            "w1": _w((lambda *s: torch.randn(*s, dtype=torch.bfloat16) * 0.05)(D, Dff)),
            "w2": _w((lambda *s: torch.randn(*s, dtype=torch.bfloat16) * 0.05)(Dff, D)),
        }
    _B_in = _B_setup()

    return {
        "A_u": _A_in["u"],
        "A_log_A_real": _A_in["log_A_real"],
        "A_A_imag": _A_in["A_imag"],
        "A_C_real": _A_in["C_real"],
        "A_C_imag": _A_in["C_imag"],
        "A_log_dt": _A_in["log_dt"],
        "A_D_skip": _A_in["D_skip"],
        "B_x": _B_in["x"],
        "B_w1": _B_in["w1"],
        "B_w2": _B_in["w2"],
    }

def reference(A_u, A_log_A_real, A_A_imag, A_C_real, A_C_imag, A_log_dt, A_D_skip, B_x, B_w1, B_w2):
    def _A_ref(u, log_A_real, A_imag, C_real, C_imag, log_dt, D_skip):
        L = u.shape[1]
        dt = torch.exp(log_dt)
        A = -torch.exp(log_A_real) + 1j * A_imag
        C = (C_real + 1j * C_imag) * dt[:, None]
        pos = (lambda x, dt: x.to(dt))(torch.arange(L), log_dt.dtype)
        P = torch.exp((A * dt[:, None])[:, :, None] * pos[None, None, :])
        K = 2.0 * torch.einsum("dn,dnl->dl", C, P).real
        L2 = 2 * L
        u_t = (lambda x, *d: x.permute(*d))(u, 0, 2, 1)
        Kf = (lambda x, *a, **k: torch.fft.rfft(x.float(), *a, **k))(K, L2)
        Uf = (lambda x, *a, **k: torch.fft.rfft(x.float(), *a, **k))(u_t, L2)
        y = (lambda x, *a, **k: torch.fft.irfft(x, *a, **k).to(torch.bfloat16))(Uf * Kf[None, :, :], L2)[:, :, :L]
        y = (lambda x, *d: x.permute(*d))(y, 0, 2, 1)
        return y + u * D_skip[None, None, :]

    def _B_ref(x, w1, w2):
        h = torch.einsum("bsd,df->bsf", x, w1)
        zero = torch.zeros_like(h)
        h = torch.maximum(h, zero)
        return torch.einsum("bsf,fd->bsd", h, w2)

    x = A_u
    xn = _layernorm(x, axis=-1)
    return x + _A_ref(xn, A_log_A_real, A_A_imag, A_C_real, A_C_imag, A_log_dt, A_D_skip) + _B_ref(xn, B_w1, B_w2)
```

```cpp
#include <hip/hip_runtime.h>
#include <hip/hip_bf16.h>
#include <stdint.h>

#define ROUND_POS 1

constexpr int L = 2048, D = 512, N = 128, F = 1792;
typedef unsigned short u16;

__device__ __forceinline__ float bf2f(u16 v) { return __uint_as_float(((uint32_t)v) << 16); }
__device__ __forceinline__ u16 f2bf(float f) { __hip_bfloat16 b = __float2bfloat16(f); return *(u16*)&b; }
__device__ __forceinline__ float rbf(float f) { return bf2f(f2bf(f)); }

__global__ void ln_kernel(const u16* x, float* xn) {
  int t = blockIdx.x; int tid = threadIdx.x;
  float v[8]; float s = 0;
  for (int i = 0; i < 8; i++) { v[i] = bf2f(x[t * D + tid * 8 + i]); s += v[i]; }
  for (int o = 32; o; o >>= 1) s += __shfl_xor(s, o);
  float m = s / D;
  float q = 0;
  for (int i = 0; i < 8; i++) { float d = v[i] - m; q += d * d; }
  for (int o = 32; o; o >>= 1) q += __shfl_xor(q, o);
  float var = rbf(q / D);
  float mb = rbf(m);
  float sd = rbf(sqrtf(rbf(var + 1e-5f)));
  for (int i = 0; i < 8; i++) xn[t * D + tid * 8 + i] = rbf(rbf(v[i] - mb) / sd);
}

__global__ void kgen_kernel(const u16* logA, const u16* Aim, const u16* Cre, const u16* Cim, const u16* logdt, float* K) {
  int d = blockIdx.x; int l = blockIdx.y * 256 + threadIdx.x;
  float dt = rbf(expf(bf2f(logdt[d])));
  float pos = rbf((float)l);
  float acc = 0;
  for (int n = 0; n < N; n++) {
    float ar = -rbf(expf(bf2f(logA[d * N + n]))) * dt;
    float ai = bf2f(Aim[d * N + n]) * dt;
    float cr = bf2f(Cre[d * N + n]) * dt, ci = bf2f(Cim[d * N + n]) * dt;
    float e = expf(ar * pos); float s, c; sincosf(ai * pos, &s, &c);
    acc += cr * e * c - ci * e * s;
  }
  K[d * L + l] = 2.f * acc;
}

__global__ void conv_kernel(const float* xn, const float* K, float* y) {
  int d = blockIdx.x * 64 + (threadIdx.x & 63); int t = blockIdx.y * 4 + (threadIdx.x >> 6);
  float acc = 0;
  for (int l = 0; l <= t; l++) acc += K[d * L + l] * xn[(t - l) * D + d];
  y[t * D + d] = acc;
}

__global__ void g1_kernel(const float* xn, const u16* w1, float* h) {
  int f = blockIdx.x * 256 + threadIdx.x; int t = blockIdx.y;
  float acc = 0;
  for (int k = 0; k < D; k++) acc += xn[t * D + k] * bf2f(w1[k * F + f]);
  h[t * F + f] = fmaxf(rbf(acc), 0.f);
}

__global__ void g2_kernel(const u16* x, const float* xn, const float* y, const u16* Dsk, const float* h, const u16* w2, u16* out) {
  int d = blockIdx.x * 256 + threadIdx.x; int t = blockIdx.y;
  float acc = 0;
  for (int k = 0; k < F; k++) acc += h[t * F + k] * bf2f(w2[k * D + d]);
  float a = rbf(rbf(y[t * D + d]) + rbf(xn[t * D + d] * bf2f(Dsk[d])));
  float r = rbf(bf2f(x[t * D + d]) + a);
  out[t * D + d] = f2bf(r + rbf(acc));
}

extern "C" void kernel_launch(void* const* d_in, const int* in_sizes, int n_in,
                              void* d_out, int out_size, void* d_ws, size_t ws_size,
                              hipStream_t stream) {
  const u16* x = (const u16*)d_in[0];
  float* ws = (float*)d_ws;
  float* xn = ws; float* K = xn + L * D; float* y = K + L * D; float* h = y + L * D;
  ln_kernel<<<L, 64, 0, stream>>>(x, xn);
  kgen_kernel<<<dim3(D, L / 256), 256, 0, stream>>>((const u16*)d_in[1], (const u16*)d_in[2], (const u16*)d_in[3], (const u16*)d_in[4], (const u16*)d_in[5], K);
  conv_kernel<<<dim3(D / 64, L / 4), 256, 0, stream>>>(xn, K, y);
  g1_kernel<<<dim3(F / 256, L), 256, 0, stream>>>(xn, (const u16*)d_in[8], h);
  g2_kernel<<<dim3(D / 256, L), 256, 0, stream>>>(x, xn, y, (const u16*)d_in[6], h, (const u16*)d_in[9], (u16*)d_out);
}
```

```cpp
#include <hip/hip_runtime.h>
#include <hip/hip_bf16.h>
#include <stdint.h>

namespace {

typedef unsigned short u16;
typedef unsigned long long u64;
typedef short s4 __attribute__((ext_vector_type(4)));
typedef short s8 __attribute__((ext_vector_type(8)));
typedef short sh2 __attribute__((ext_vector_type(2)));
typedef __bf16 b2 __attribute__((ext_vector_type(2)));
typedef _Float16 h2 __attribute__((ext_vector_type(2)));
typedef _Float16 h8 __attribute__((ext_vector_type(8)));
typedef float f2 __attribute__((ext_vector_type(2)));
typedef float f4 __attribute__((ext_vector_type(4)));
typedef float f16v __attribute__((ext_vector_type(16)));
typedef unsigned v2u __attribute__((ext_vector_type(2)));
typedef unsigned v4u __attribute__((ext_vector_type(4)));
#define LAS __attribute__((address_space(3)))
#define GAS __attribute__((address_space(1)))
#define DI __device__ __forceinline__

constexpr int L_ = 2048, D_ = 512, F_ = 1792, N_ = 128;

constexpr int WS_EPOCH = 0;
constexpr int WS_HFLAG = 4096;
constexpr int WS_XFLAG = WS_HFLAG + 64 * 128;
constexpr int WS_YFLAG = WS_XFLAG + 32 * 128;
constexpr int WS_GRAN = 32768;
constexpr int WS_XT = 65536;
constexpr int WS_YT = WS_XT + 2 * 1024 * 1024;
constexpr int WS_H = WS_YT + 2 * 1024 * 1024;
constexpr int WS_END = WS_H + 64 * 112 * 1024;
constexpr int WS_STAMP = 16 * 1024 * 1024;

constexpr int LD_KT = 131072;
constexpr int KT_COPY = 4352 + 64;
constexpr int KT_CH = 2 * KT_COPY;
constexpr int LD_UT = LD_KT + 2 * KT_CH;
constexpr int UT_CH = 4576;
constexpr int LD_CTL = LD_UT + 2 * UT_CH;
constexpr int LD_LPROG = LD_CTL, LD_GPROG = LD_CTL + 16, LD_XPROG = LD_CTL + 32, LD_TAG = LD_CTL + 48;
constexpr int LD_CSP = LD_CTL + 64;
constexpr int LD_MEAN = LD_CSP + 2048;
constexpr int LD_RS = LD_MEAN + 512;
constexpr int LD_END = LD_RS + 256;
constexpr int LDS_BYTES = 163840;
static_assert(LD_END <= LDS_BYTES, "lds");
static_assert(LD_UT % 16 == 0 && LD_CTL % 16 == 0 && KT_COPY % 16 == 0, "align");

constexpr unsigned kLdsSpin = 1u << 22, kMemSpin = 1u << 17;

DI float bf2f(unsigned v) { return __uint_as_float(v << 16); }
DI float bflo(unsigned v) { return __uint_as_float(v << 16); }
DI float bfhi(unsigned v) { return __uint_as_float(v & 0xffff0000u); }
DI unsigned pkbf(float a, float b) { f2 v = {a, b}; return __builtin_bit_cast(unsigned, __builtin_convertvector(v, b2)); }
DI unsigned pkh(float a, float b) { f2 v = {a, b}; return __builtin_bit_cast(unsigned, __builtin_convertvector(v, h2)); }
DI float rbf(float a) { return bflo(pkbf(a, a)); }
DI float dot2ones(unsigned v, float acc) {
  return __builtin_amdgcn_fdot2_f32_bf16(__builtin_bit_cast(b2, v), __builtin_bit_cast(b2, 0x3f803f80u), acc, false);
}
template <class T> DI T lds_ld(unsigned a) { return *(LAS T*)(a); }
template <class T> DI void lds_st(unsigned a, T v) { *(LAS T*)(a) = v; }
DI s4 tr_ld(unsigned a) { return __builtin_amdgcn_ds_read_tr16_b64_v4i16((LAS s4*)(a)); }
DI s8 cat(s4 a, s4 b) { return __builtin_shufflevector(a, b, 0, 1, 2, 3, 4, 5, 6, 7); }
DI f16v mfma32(s8 a, s8 b, f16v c) { return __builtin_amdgcn_mfma_f32_32x32x16_bf16(a, b, c, 0, 0, 0); }
DI f4 mfma16(s8 a, s8 b, f4 c) { return __builtin_amdgcn_mfma_f32_16x16x32_bf16(a, b, c, 0, 0, 0); }
DI f4 mfma16h(v4u a, v4u b, f4 c) {
  return __builtin_amdgcn_mfma_f32_16x16x32_f16(__builtin_bit_cast(h8, a), __builtin_bit_cast(h8, b), c, 0, 0, 0);
}
#define SCHED() __builtin_amdgcn_sched_barrier(0)
#define CFENCE() asm volatile("" ::: "memory")
#define WAITV(n) asm volatile("s_waitcnt vmcnt(" #n ")" ::: "memory")

DI void set_prog(unsigned addr, unsigned v) { asm volatile("ds_write_b32 %0, %1" ::"v"(addr), "v"(v) : "memory"); }
DI void wait_prog(unsigned addr4, unsigned need) {
#pragma unroll 1
  for (unsigned i = 0; i < kLdsSpin; ++i) {
    v4u v;
    asm volatile("ds_read_b128 %0, %1\n\ts_waitcnt lgkmcnt(0)" : "=v"(v) : "v"(addr4) : "memory");
    unsigned m = min(min(v.x, v.y), min(v.z, v.w));
    if (__builtin_amdgcn_readfirstlane(m) >= need) break;
    __builtin_amdgcn_s_sleep(1);
  }
}
DI void wait_one(unsigned addr, unsigned need) {
#pragma unroll 1
  for (unsigned i = 0; i < kLdsSpin; ++i) {
    unsigned v;
    asm volatile("ds_read_b32 %0, %1\n\ts_waitcnt lgkmcnt(0)" : "=v"(v) : "v"(addr) : "memory");
    if (__builtin_amdgcn_readfirstlane(v) >= need) break;
    __builtin_amdgcn_s_sleep(1);
  }
}
DI void glds(unsigned voff, const char* sbase, unsigned m0v) {
  asm volatile("s_mov_b32 m0, %2\n\ts_nop 0\n\tglobal_load_lds_dwordx4 %0, %1" ::"v"(voff), "s"(sbase), "s"(m0v) : "memory");
}
constexpr int SC1 = 16;
typedef __amdgpu_buffer_rsrc_t rsrc_t;
DI v4u ld128(rsrc_t r, int voff, int soff) { return __builtin_amdgcn_raw_buffer_load_b128(r, voff, soff, SC1); }
DI unsigned ld32(rsrc_t r, int voff, int soff) { return __builtin_amdgcn_raw_buffer_load_b32(r, voff, soff, SC1); }
DI void st128(rsrc_t r, v4u v, int voff, int soff) { __builtin_amdgcn_raw_buffer_store_b128(v, r, voff, soff, SC1); }
DI void st32(rsrc_t r, unsigned v, int voff, int soff) { __builtin_amdgcn_raw_buffer_store_b32(v, r, voff, soff, SC1); }
DI void poll_tag(rsrc_t r, int voff, unsigned tag) {
#pragma unroll 1
  for (unsigned i = 0; i < kMemSpin; ++i) {
    CFENCE();
    const unsigned v = ld32(r, voff, 0);
    if (__all(v == tag)) break;
    __builtin_amdgcn_s_sleep(8);
  }
  CFENCE();
}
template <int CTRL> DI float dpp_add(float v) {
  return v + __builtin_bit_cast(float, __builtin_amdgcn_update_dpp(0, __builtin_bit_cast(int, v), CTRL, 0xf, 0xf, true));
}
DI float sum8(float v) {
  v = dpp_add<0xB1>(v); v = dpp_add<0x4E>(v); return dpp_add<0x141>(v);
}
DI float lane_f(float v, int l) { return __builtin_bit_cast(float, __builtin_amdgcn_readlane(__builtin_bit_cast(int, v), l)); }
DI float wave_sum(float v) {
  v = dpp_add<0x140>(sum8(v));
  return (lane_f(v, 0) + lane_f(v, 16)) + (lane_f(v, 32) + lane_f(v, 48));
}
template <int CTRL> DI float dpp_max(float v) {
  return fmaxf(v, __builtin_bit_cast(float, __builtin_amdgcn_update_dpp(0, __builtin_bit_cast(int, v), CTRL, 0xf, 0xf, true)));
}
DI float wave_max(float v) {
  v = dpp_max<0x140>(dpp_max<0x141>(dpp_max<0x4E>(dpp_max<0xB1>(v))));
  return fmaxf(fmaxf(lane_f(v, 0), lane_f(v, 16)), fmaxf(lane_f(v, 32), lane_f(v, 48)));
}
struct cpx { float r, i; };
DI float fmul1(float a, float b) { float r; asm("v_mul_f32 %0, %1, %2" : "=v"(r) : "v"(a), "v"(b)); return r; }
DI float ffma1(float a, float b, float c) { float r; asm("v_fma_f32 %0, %1, %2, %3" : "=v"(r) : "v"(a), "v"(b), "v"(c)); return r; }
DI float fnma1(float a, float b, float c) { float r; asm("v_fma_f32 %0, -%1, %2, %3" : "=v"(r) : "v"(a), "v"(b), "v"(c)); return r; }
DI cpx cmul(cpx a, cpx b) { return {fnma1(a.i, b.i, fmul1(a.r, b.r)), ffma1(a.i, b.r, fmul1(a.r, b.i))}; }
DI cpx cexp_at(float lr, float rev, float p) {
  const float e = __builtin_amdgcn_exp2f(lr * p);
  const float f = __builtin_amdgcn_fractf(rev * p);
  return {e * __builtin_amdgcn_cosf(f), e * __builtin_amdgcn_sinf(f)};
}

#define STAMP(i)

__global__ __launch_bounds__(768, 3) void fused_kernel(
    const u16* __restrict__ x, const u16* __restrict__ logA, const u16* __restrict__ Aim, const u16* __restrict__ Cre,
    const u16* __restrict__ Cim, const u16* __restrict__ logdt, const u16* __restrict__ Dsk, const u16* __restrict__ w1,
    const u16* __restrict__ w2, u16* __restrict__ out, char* ws) {
  extern __shared__ __attribute__((aligned(16))) char smem[];
  const unsigned lds0 = (unsigned)(size_t)(LAS char*)smem;
  const int lane = threadIdx.x & 63;
  const int w = __builtin_amdgcn_readfirstlane(threadIdx.x >> 6), role = w >> 2, rw = w & 3;
  const int b = blockIdx.x, xcd = b & 7, idx = b >> 3;
  const bool has1 = idx < 28;
  const int panel = 4 * (xcd >> 1) + (idx & 3), slice = 7 * (xcd & 1) + (idx >> 2);
  const int tb = 4 * xcd + (idx & 3), cb = idx >> 2;
  const int rot2 = (7 * (2 * (idx & 3) + cb) / 4) % 14;
  const rsrc_t rs = __builtin_amdgcn_make_buffer_rsrc(ws, 0, WS_END, 0x00020000);
  const int hh = lane >> 5, blk = (lane >> 4) & 1, q = (lane & 15) >> 2, pp = lane & 3, g = lane >> 4, n16 = lane & 15;
  STAMP(0);
  if (threadIdx.x < 16) lds_st<unsigned>(lds0 + LD_CTL + threadIdx.x * 4, 0u);
  __syncthreads();

  if (role == 0) {
    __builtin_amdgcn_s_setprio(2);
    const unsigned myprog = lds0 + LD_GPROG + rw * 4;
    if (has1) {
      const int tw = rw >> 1, fw = rw & 1, n = lane & 31;
      unsigned ax[4], aw[2];
#pragma unroll
      for (int s = 0; s < 4; s++) ax[s] = lds0 + (64 * tw + n) * 128 + (((2 * s + hh) ^ ((n >> 1) & 7)) << 4);
#pragma unroll
      for (int ft = 0; ft < 2; ft++) aw[ft] = lds0 + 16384 + (8 * hh + q) * 256 + (((2 * fw + ft) ^ q) << 6) + 32 * blk + 8 * pp;
      f16v acc[2][2];
#pragma unroll
      for (int i = 0; i < 16; i++) acc[0][0][i] = acc[0][1][i] = acc[1][0][i] = acc[1][1][i] = 0.f;
      s8 fx[2][2], fw1[2][2];
      auto rd = [&](int buf, int s, unsigned so) {
#pragma unroll
        for (int tt = 0; tt < 2; tt++) fx[buf][tt] = lds_ld<s8>(ax[s] + so + tt * 4096);
#pragma unroll
        for (int ft = 0; ft < 2; ft++) fw1[buf][ft] = cat(tr_ld(aw[ft] + so + s * 4096), tr_ld(aw[ft] + so + s * 4096 + 1024));
      };
      auto mm = [&](int buf) {
#pragma unroll
        for (int ft = 0; ft < 2; ft++)
#pragma unroll
          for (int tt = 0; tt < 2; tt++) acc[ft][tt] = mfma32(fw1[buf][ft], fx[buf][tt], acc[ft][tt]);
      };
      wait_prog(lds0 + LD_LPROG, 1);
      STAMP(1);
      rd(0, 0, 0);
#pragma unroll 1
      for (int sb = 0; sb < 8; sb++) {
        const unsigned so = (sb & 3) * 32768;
        rd(1, 1, so); SCHED(); mm(0); SCHED();
        rd(0, 2, so); SCHED(); mm(1); SCHED();
        rd(1, 3, so); SCHED();
        set_prog(myprog, sb + 1);
        mm(0); SCHED();
        if (sb < 7) {
          wait_prog(lds0 + LD_LPROG, sb + 2);
          rd(0, 0, ((sb + 1) & 3) * 32768);
        }
        SCHED(); mm(1); SCHED();
      }
      wait_prog(lds0 + LD_LPROG, 9);
      STAMP(2);
      {
        s8 ca[2], cm[2];
#pragma unroll
        for (int ft = 0; ft < 2; ft++) {
          const f4 p4 = lds_ld<f4>(lds0 + LD_CSP + (64 * fw + 32 * ft + n) * 16);
          const float cs = (p4[0] + p4[1]) + (p4[2] + p4[3]);
          const float c0 = rbf(cs), c1 = rbf(cs - c0), c2 = cs - c0 - c1;
          v4u a = {pkbf(c0, c1), pkbf(c2, 0.f), 0u, 0u};
          if (hh) a = v4u{0u, 0u, 0u, 0u};
          ca[ft] = __builtin_bit_cast(s8, a);
        }
#pragma unroll
        for (int tt = 0; tt < 2; tt++) {
          const unsigned m = lds_ld<unsigned>(lds0 + LD_MEAN + (64 * tw + 32 * tt + n) * 4);
          v4u a = {m, m & 0xffffu, 0u, 0u};
          if (hh) a = v4u{0u, 0u, 0u, 0u};
          cm[tt] = __builtin_bit_cast(s8, a);
        }
#pragma unroll
        for (int ft = 0; ft < 2; ft++)
#pragma unroll
          for (int tt = 0; tt < 2; tt++) acc[ft][tt] = mfma32(ca[ft], cm[tt], acc[ft][tt]);
      }
      const int hb = WS_H + ((panel * 4 + 2 * tw) * 112 + slice * 8 + 4 * fw) * 1024;
#pragma unroll
      for (int tt = 0; tt < 2; tt++)
#pragma unroll
        for (int ft = 0; ft < 2; ft++)
#pragma unroll
          for (int s2 = 0; s2 < 2; s2++) {
            v4u o;
#pragma unroll
            for (int j = 0; j < 4; j++) {
              const unsigned p = pkbf(acc[ft][tt][8 * s2 + 2 * j], acc[ft][tt][8 * s2 + 2 * j + 1]);
              o[j] = __builtin_bit_cast(unsigned, __builtin_elementwise_max(__builtin_bit_cast(sh2, p), sh2{0, 0}));
            }
            st128(rs, o, lane * 16 + (2 * ft + s2) * 1024, hb + tt * 112 * 1024);
          }
    } else {
      set_prog(myprog, 8);
    }
    wait_one(lds0 + LD_XPROG, 1);
    const unsigned tag = lds_ld<unsigned>(lds0 + LD_TAG);
    if (has1) {
      WAITV(0);
      if (lane == 0) st32(rs, tag, WS_HFLAG + (panel * 4 + rw) * 128 + slice * 4, 0);
    }
    STAMP(3);
    const int th = rw >> 1, kh = rw & 1;
    poll_tag(rs, WS_HFLAG + ((tb >> 1) * 4 + (tb & 1) * 2 + kh) * 128 + min(lane, 13) * 4, tag);
    STAMP(4);
    const int hb2 = WS_H + ((2 * tb + th) * 112 + 4 * kh) * 1024;
    constexpr int WIN = 4;
    v4u hf[WIN][4];
    auto ldh = [&](int c) {
      int sg = c + rot2; sg = sg >= 14 ? sg - 14 : sg;
#pragma unroll
      for (int s = 0; s < 4; s++) hf[c % WIN][s] = ld128(rs, lane * 16 + s * 1024, hb2 + sg * 8192);
    };
#pragma unroll
    for (int c = 0; c < WIN; c++) ldh(c);
    SCHED();
    unsigned a2[2];
#pragma unroll
    for (int ct = 0; ct < 2; ct++) a2[ct] = lds0 + (64 * kh + 4 * hh + q) * 128 + ((ct ^ (q >> 1)) << 6) + 32 * blk + 8 * pp;
    f16v ac2[2];
#pragma unroll
    for (int i = 0; i < 16; i++) ac2[0][i] = ac2[1][i] = 0.f;
#pragma unroll
    for (int c = 0; c < 14; c++) {
      wait_prog(lds0 + LD_LPROG, 10 + c);
      s8 fw2[4][2];
      const unsigned so = (c & 7) * 16384;
#pragma unroll
      for (int s = 0; s < 4; s++)
#pragma unroll
        for (int ct = 0; ct < 2; ct++) fw2[s][ct] = cat(tr_ld(a2[ct] + so + s * 2048), tr_ld(a2[ct] + so + s * 2048 + 1024));
      set_prog(myprog, 9 + c);
      SCHED();
#pragma unroll
      for (int s = 0; s < 4; s++)
#pragma unroll
        for (int ct = 0; ct < 2; ct++) ac2[ct] = mfma32(fw2[s][ct], __builtin_bit_cast(s8, hf[c % WIN][s]), ac2[ct]);
      SCHED();
      if (c + WIN < 14) ldh(c + WIN);
      SCHED();
    }
    STAMP(5);
    wait_prog(lds0 + LD_GPROG, 16);
    const unsigned ex = lds0 + 6 * 16384;
    f16v keep;
    if (kh == 0) {
#pragma unroll
      for (int j = 0; j < 4; j++) lds_st<f4>(ex + rw * 4096 + j * 1024 + lane * 16, f4{ac2[1][4 * j], ac2[1][4 * j + 1], ac2[1][4 * j + 2], ac2[1][4 * j + 3]});
      keep = ac2[0];
    } else {
#pragma unroll
      for (int j = 0; j < 4; j++) lds_st<f4>(ex + rw * 4096 + j * 1024 + lane * 16, f4{ac2[0][4 * j], ac2[0][4 * j + 1], ac2[0][4 * j + 2], ac2[0][4 * j + 3]});
      keep = ac2[1];
    }
    set_prog(myprog, 23);
    wait_prog(lds0 + LD_XPROG, 8);
    wait_one(lds0 + LD_GPROG + (rw ^ 1) * 4, 23);
    const float rstd = lds_ld<float>(lds0 + LD_RS + (32 * th + (lane & 31)) * 4);
    const unsigned ay = lds0 + LD_UT + (32 * kh + 4 * hh + q) * 128 + ((th ^ (q >> 1)) << 6) + 32 * blk + 8 * pp;
    v2u pk[4];
#pragma unroll
    for (int j = 0; j < 4; j++) {
      const f4 o = lds_ld<f4>(ex + (rw ^ 1) * 4096 + j * 1024 + lane * 16);
      const v2u y = __builtin_bit_cast(v2u, tr_ld(ay + j * 1024));
      pk[j].x = pkbf(bflo(y.x) + rstd * (keep[4 * j] + o[0]), bfhi(y.x) + rstd * (keep[4 * j + 1] + o[1]));
      pk[j].y = pkbf(bflo(y.y) + rstd * (keep[4 * j + 2] + o[2]), bfhi(y.y) + rstd * (keep[4 * j + 3] + o[3]));
    }
    u16* op = out + (size_t)(64 * tb + 32 * th + (lane & 31)) * D_ + 64 * cb + 32 * kh + 8 * hh;
#pragma unroll
    for (int j = 0; j < 4; j += 2) {
      const auto r0 = __builtin_amdgcn_permlane32_swap(pk[j].x, pk[j + 1].x, false, false);
      const auto r1 = __builtin_amdgcn_permlane32_swap(pk[j].y, pk[j + 1].y, false, false);
      const unsigned e0 = r0[0], e1 = r1[0], e2 = r0[1], e3 = r1[1];
      *(v4u*)(op + 8 * j) = v4u{e0, e1, e2, e3};
    }
    STAMP(6);
  } else if (role == 1) {
    __builtin_amdgcn_s_setprio(3);
    const unsigned myprog = lds0 + LD_LPROG + rw * 4;
    v4u wq[6][4];
    const unsigned w2off = (unsigned)((32 * rw + (lane >> 3)) * 1024 + cb * 128 + (lane & 7) * 16);
    const rsrc_t rw2 = __builtin_amdgcn_make_buffer_rsrc((void*)w2, 0, F_ * D_ * 2, 0x00020000);
    auto ldw2 = [&](int c) {
      int sg = c + rot2; sg = sg >= 14 ? sg - 14 : sg;
#pragma unroll
      for (int j = 0; j < 4; j++) wq[c % 6][j] = __builtin_amdgcn_raw_buffer_load_b128(rw2, w2off, sg * 131072 + j * 8192, 0);
      CFENCE();
    };
    {
      const int rot1 = ((idx >> 2) + 2 * (idx & 3)) & 7;
      unsigned xo[2];
#pragma unroll
      for (int j = 0; j < 2; j++) xo[j] = (unsigned)((8 * j + (lane >> 3)) * 1024 + (((lane & 7) ^ ((4 * j + g) & 7)) << 4));
      const unsigned wo = (unsigned)(g * 3584 + ((n16 ^ (g << 2)) << 4));
      const char* xb = (const char*)x + (size_t)(panel * 128 + 32 * rw) * 1024;
      const char* wb = (const char*)w1 + (size_t)(16 * rw) * 3584 + slice * 256;
      auto issue_x = [&](int s) {
        const int ss = (s + rot1) & 7;
#pragma unroll
        for (int j = 0; j < 4; j++) glds(xo[j & 1], xb + ss * 128 + (j >> 1) * 16384, lds0 + (s & 3) * 32768 + rw * 4096 + j * 1024);
      };
      auto issue_w = [&](int s) {
        const int ss = (s + rot1) & 7;
#pragma unroll
        for (int j = 0; j < 4; j++) glds(wo, wb + (size_t)(ss * 64 + 4 * j) * 3584, lds0 + (s & 3) * 32768 + 16384 + rw * 4096 + j * 1024);
      };
      float rsum[4] = {0.f, 0.f, 0.f, 0.f}, csum[2] = {0.f, 0.f};
      unsigned ac[2];
#pragma unroll
      for (int u = 0; u < 2; u++) ac[u] = lds0 + 16384 + rw * 4096 + q * 256 + (((2 * u + (g >> 1)) ^ q) << 6) + (g & 1) * 32 + 8 * pp;
      auto sums = [&](int s) {
        const unsigned so = (s & 3) * 32768;
#pragma unroll
        for (int j = 0; j < 4; j++) {
          const v4u v = lds_ld<v4u>(lds0 + so + rw * 4096 + j * 1024 + lane * 16);
          rsum[j] = dot2ones(v.w, dot2ones(v.z, dot2ones(v.y, dot2ones(v.x, rsum[j]))));
#pragma unroll
          for (int u = 0; u < 2; u++) {
            const v2u t = __builtin_bit_cast(v2u, tr_ld(ac[u] + so + j * 1024));
            csum[u] = dot2ones(t.y, dot2ones(t.x, csum[u]));
          }
        }
      };
      if (has1) {
      issue_x(0); issue_w(0);
      issue_x(1); issue_w(1);
#pragma unroll 1
      for (int s = 0; s < 6; s++) {
        WAITV(8);
        set_prog(myprog, s + 1);
        if (s == 0) STAMP(1);
        if (s >= 2) wait_prog(lds0 + LD_GPROG, s - 1);
        issue_x(s + 2);
        sums(s);
        CFENCE();
        issue_w(s + 2);
      }
      WAITV(8);
      set_prog(myprog, 7);
      }
      ldw2(0);
      if (has1) sums(6);
      CFENCE();
      ldw2(1);
      if (has1) {
      WAITV(8);
      set_prog(myprog, 8);
      STAMP(2);
      sums(7);
#pragma unroll
      for (int u = 0; u < 2; u++) lds_st<float>(lds0 + LD_CSP + (64 * u + lane) * 16 + rw * 4, csum[u]);
#pragma unroll
      for (int j = 0; j < 4; j++) {
        const float m = sum8(rsum[j]) * (-1.f / D_);
        if ((lane & 7) == 0) lds_st<unsigned>(lds0 + LD_MEAN + (32 * rw + 8 * j + (lane >> 3)) * 4, pkbf(m, m));
      }
      set_prog(myprog, 9);
      }
    }
    ldw2(2); ldw2(3); ldw2(4); ldw2(5);
    STAMP(3);
    const unsigned wdst = lds0 + rw * 4096 + (lane >> 3) * 128 + (((lane & 7) ^ (((lane >> 4) & 1) << 2)) << 4);
#pragma unroll
    for (int c = 0; c < 14; c++) {
      wait_prog(lds0 + LD_GPROG, c < 8 ? 5 + (c >> 1) : c + 1);
#pragma unroll
      for (int j = 0; j < 4; j++) lds_st<v4u>(wdst + (c & 7) * 16384 + j * 1024, wq[c % 6][j]);
      set_prog(myprog, 10 + c);
      if (c + 6 < 14) ldw2(c + 6);
      if (c == 7) STAMP(4);
    }
    STAMP(5);
  } else {
    const unsigned myprog = lds0 + LD_XPROG + rw * 4, xprog = lds0 + LD_XPROG;
    const int cp = b, c = rw >> 1, mh = rw & 1, ch = 2 * cp + c;
    const int st0 = 256 * xcd + 8 * idx + 2 * rw;
    const v4u r0 = *(const v4u*)(x + (size_t)st0 * D_ + lane * 8), r1 = *(const v4u*)(x + (size_t)(st0 + 1) * D_ + lane * 8);
    const u16* xt = x + (size_t)(64 * tb + (lane >> 1)) * D_ + 64 * cb + 16 * rw + (lane & 1) * 8;
    const v4u t0 = *(const v4u*)xt, t1 = *(const v4u*)(xt + 32 * D_);
    const int pi = ch * N_ + 64 * mh + lane;
    const unsigned pA = logA[pi], pI = Aim[pi], pCr = Cre[pi], pCi = Cim[pi], pdt = logdt[ch], pD = Dsk[ch];
    const unsigned scr = lds0 + LD_KT + c * KT_CH + mh * KT_COPY;
    unsigned tag;
    asm volatile("s_load_dword %0, %1, 0x0\n\ts_waitcnt lgkmcnt(0)" : "=s"(tag) : "s"(ws) : "memory");
    tag += 1u;
    lds_st<unsigned>(lds0 + LD_TAG, tag);
    set_prog(myprog, 1);
    STAMP(1);
    {
      unsigned gr[2];
#pragma unroll
      for (int k = 0; k < 2; k++) {
        const v4u r = k ? r1 : r0;
        float v[8];
#pragma unroll
        for (int j = 0; j < 4; j++) { v[2 * j] = bflo(r[j]); v[2 * j + 1] = bfhi(r[j]); }
        const float m = wave_sum(((v[0] + v[1]) + (v[2] + v[3])) + ((v[4] + v[5]) + (v[6] + v[7]))) * (1.f / D_);
        float qq = 0.f;
#pragma unroll
        for (int j = 0; j < 8; j++) { const float d = v[j] - m; qq += d * d; }
        const float var = rbf(wave_sum(qq) * (1.f / D_));
        gr[k] = pkbf(m, sqrtf(rbf(var + 1e-5f)));
      }
      const int sbk = st0 >> 3;
      if (lane == 0) st128(rs, v4u{gr[0], tag, gr[1], tag}, WS_GRAN + (((sbk >> 6) * 4 + rw) * 64 + (sbk & 63)) * 16, 0);
    }
    v4u xk[2];
    {
      lds_st<v4u>(scr + lane * 16, t0);
      lds_st<v4u>(scr + 1024 + lane * 16, t1);
      CFENCE();
      const int cho = 64 * cb + 16 * rw + n16;
#pragma unroll
      for (int j = 0; j < 2; j++) {
        const unsigned a = scr + (32 * j + 8 * g + q) * 32 + 8 * pp;
        xk[j] = __builtin_bit_cast(v4u, cat(tr_ld(a), tr_ld(a + 128)));
        st128(rs, xk[j], WS_XT + (cho >> 1) * 8192 + (2 * tb + j) * 128 + (cho & 1) * 64 + g * 16, 0);
      }
      CFENCE();
    }
    STAMP(2);
    f4 kd[3];
    {
      const float dt = rbf(__expf(bf2f(pdt)));
      const float lr = -rbf(__expf(bf2f(pA))) * dt * 1.44269504088896341f;
      const float rev = bf2f(pI) * dt * 0.15915494309189535f;
      cpx C = {bf2f(pCr) * dt, bf2f(pCi) * dt};
      const float mx = wave_max(fmaxf(fabsf(C.r), fabsf(C.i)));
      const unsigned eb = min(max(__float_as_uint(mx) >> 23, 2u), 252u);
      const float sdn = __uint_as_float((253u - eb) << 23), sup = __uint_as_float((eb + 2u) << 23);
      C.r *= sdn; C.i *= sdn;
      const cpx e1 = cexp_at(lr, rev, 1.f), e2 = cmul(e1, e1), e4 = cmul(e2, e2);
      const cpx e8 = cexp_at(lr, rev, 8.f), gg = cexp_at(lr, rev, 128.f);
      const unsigned wa = scr + lane * 4, ra = scr + n16 * 272 + g * 16;
      auto putQ = [&](int i, cpx v) { lds_st<unsigned>(wa + i * 272, pkh(v.r, v.i)); };
      auto putP = [&](int i, cpx v) { lds_st<unsigned>(wa + i * 272, pkh(v.r, -v.i)); };
      v4u fq[4];
      {
        const cpx e16 = cmul(e8, e8);
        cpx a = {1.f, 0.f}, bq = e8;
#pragma unroll
        for (int j = 0; j < 16; j += 2) {
          putQ(j, a); putQ(j + 1, bq);
          if (j < 14) { a = cmul(a, e16); bq = cmul(bq, e16); }
        }
        CFENCE();
#pragma unroll
        for (int s = 0; s < 4; s++) fq[s] = lds_ld<v4u>(ra + s * 64);
        CFENCE();
      }
      auto tile = [&](int T) {
        CFENCE();
        f4 a = {0.f, 0.f, 0.f, 0.f};
#pragma unroll
        for (int s = 0; s < 4; s++) a = mfma16h(lds_ld<v4u>(ra + s * 64), fq[s], a);
        CFENCE();
        kd[T] = a * sup;
      };
      cpx P = C;
      {
        cpx a = P, bq = cmul(P, gg);
        P = cmul(bq, gg);
#pragma unroll
        for (int j = 0; j < 8; j++) {
          putP(j, a); putP(8 + j, bq);
          if (j < 7) { a = cmul(a, e1); bq = cmul(bq, e1); }
        }
      }
      tile(0);
      {
        cpx a = P, bq = cmul(P, gg);
        P = cmul(bq, gg);
#pragma unroll
        for (int j = 0; j < 4; j++) {
          putP(j, a); putP(4 + j, bq);
          if (j < 3) { a = cmul(a, e2); bq = cmul(bq, e2); }
        }
#pragma unroll
        for (int j = 0; j < 4; j++) {
          putP(8 + 2 * j, P); putP(9 + 2 * j, cmul(P, e4));
          P = cmul(P, gg);
        }
      }
      tile(1);
#pragma unroll
      for (int j = 0; j < 9; j++) {
        putP(j, P);
        if (j < 8) P = cmul(P, gg);
      }
      tile(2);
    }
    WAITV(0);
    if (lane == 0) st32(rs, tag, WS_XFLAG + ((cb * 4 + rw) * 32 + tb) * 4, 0);
    STAMP(3);
    const unsigned ut = lds0 + LD_UT + c * UT_CH;
    {
      const unsigned oscr = lds0 + LD_KT + c * KT_CH + (mh ^ 1) * KT_COPY;
      if (mh == 0) { lds_st<f4>(scr + lane * 16, kd[1]); lds_st<f4>(scr + 1024 + lane * 16, kd[2]); }
      else lds_st<f4>(scr + lane * 16, kd[0]);
      set_prog(myprog, 2);
      wait_one(xprog + (rw ^ 1) * 4, 2);
      if (mh == 0) {
        f4 v = kd[0] + lds_ld<f4>(oscr + lane * 16);
        if (lane == 0) v[0] += bf2f(pD);
        const unsigned a = ut + 2 * (128 * (g >> 1) + 4 * (g & 1) + 8 * n16);
        lds_st<v2u>(a, v2u{pkbf(v[0], v[1]), pkbf(v[2], v[3])});
      } else {
        const f4 v = kd[1] + lds_ld<f4>(oscr + lane * 16), v2 = kd[2] + lds_ld<f4>(oscr + 1024 + lane * 16);
#pragma unroll
        for (int r = 0; r < 4; r++) {
          const int p1 = (g < 2 ? 128 * (2 + g) + 2 * r : 128 * (2 * g + (r >> 1)) + 4 * (r & 1)) + 8 * n16;
          lds_st<u16>(ut + 2 * p1, (u16)pkbf(v[r], v[r]));
          const int i = 4 * g + r;
          if (i < 9) lds_st<u16>(ut + 2 * (128 * (8 + i) + 8 * n16), (u16)pkbf(v2[r], v2[r]));
        }
      }
      set_prog(myprog, 3);
      wait_one(xprog + (rw ^ 1) * 4, 3);
      const unsigned kt = lds0 + LD_KT + c * KT_CH;
#pragma unroll
      for (int it = 0; it < 8; it++) {
        const int y = 512 * mh + 64 * it + lane;
        const float l0 = (float)(2 * (2047 - 2 * y));
        const unsigned p01 = pkbf(l0, l0 - 2.f), p2 = pkbf(fmaxf(l0 - 4.f, 0.f), 0.f);
        const unsigned k0 = lds_ld<u16>(ut + (unsigned)bflo(p01)), k1 = lds_ld<u16>(ut + (unsigned)bfhi(p01));
        unsigned k2 = lds_ld<u16>(ut + (unsigned)bflo(p2));
        if (y == 1023) k2 = 0;
        lds_st<unsigned>(kt + 4 * y, k0 | (k1 << 16));
        lds_st<unsigned>(kt + KT_COPY + 4 * y, k1 | (k2 << 16));
      }
      if (lane < 8) lds_st<unsigned>(kt + mh * KT_COPY + 4096 + lane * 4, 0u);
      set_prog(myprog, 4);
    }
    STAMP(4);
    {
      v4u gq[4];
      const int fo = WS_XFLAG + (((cp >> 5) * 4 + ((cp >> 3) & 3)) * 32 + 8 * rw + (lane & 7)) * 4;
#pragma unroll 1
      for (unsigned i = 0; i < kMemSpin; ++i) {
        CFENCE();
        const unsigned f = ld32(rs, fo, 0);
        bool ok = f == tag;
#pragma unroll
        for (int j = 0; j < 4; j++) {
          gq[j] = ld128(rs, WS_GRAN + ((rw * 4 + j) * 64 + lane) * 16, 0);
          ok = ok && gq[j].y == tag && gq[j].w == tag;
        }
        if (__all(ok)) break;
        __builtin_amdgcn_s_sleep(8);
      }
      CFENCE();
      const int tok0 = 512 * rw + 8 * lane;
      v4u xv[2];
#pragma unroll
      for (int k = 0; k < 2; k++) xv[k] = ld128(rs, WS_XT + cp * 8192 + (tok0 >> 5) * 128 + k * 64 + (tok0 & 31) * 2, 0);
      float mu[8], ri[8];
#pragma unroll
      for (int j = 0; j < 4; j++) {
        mu[2 * j] = bflo(gq[j].x); ri[2 * j] = __builtin_amdgcn_rcpf(bfhi(gq[j].x));
        mu[2 * j + 1] = bflo(gq[j].z); ri[2 * j + 1] = __builtin_amdgcn_rcpf(bfhi(gq[j].z));
      }
      wait_prog(xprog, 4);
      if ((tok0 >> 6) == tb) {
        lds_st<f4>(lds0 + LD_RS + (tok0 & 63) * 4, f4{ri[0], ri[1], ri[2], ri[3]});
        lds_st<f4>(lds0 + LD_RS + (tok0 & 63) * 4 + 16, f4{ri[4], ri[5], ri[6], ri[7]});
      }
#pragma unroll
      for (int k = 0; k < 2; k++) {
        v4u o;
#pragma unroll
        for (int j = 0; j < 4; j++) {
          const unsigned d = pkbf(bflo(xv[k][j]) - mu[2 * j], bfhi(xv[k][j]) - mu[2 * j + 1]);
          o[j] = pkbf(bflo(d) * ri[2 * j], bfhi(d) * ri[2 * j + 1]);
        }
        lds_st<v4u>(lds0 + LD_UT + k * UT_CH + 480 + tok0 * 2, o);
      }
      if (mh == 0 && lane < 30) lds_st<v4u>(ut + lane * 16, v4u{0u, 0u, 0u, 0u});
      set_prog(myprog, 5);
      wait_prog(xprog, 5);
      if (b == 0 && rw == 0 && lane == 0) st32(rs, tag, WS_EPOCH, 0);
    }
    STAMP(5);
    f4 ya[8];
#pragma unroll
    for (int i = 0; i < 8; i++) ya[i] = f4{0.f, 0.f, 0.f, 0.f};
    {
      const unsigned ka = lds0 + LD_KT + c * KT_CH + (1 - (n16 & 1)) * KT_COPY + 4 * (1015 - (n16 >> 1) + 4 * g) - 64 * 56;
      const unsigned ua = ut + 2 * (224 + 16 * n16 + 8 * g);
#pragma unroll 1
      for (int r = 4 * mh; r < 4 * mh + 4; r++) {
        s8 kf[8], uf[8];
#pragma unroll
        for (int k = 0; k < 8; k++) {
          const unsigned a = ka - 64 * r + 512 * (7 - k);
          typedef unsigned v2a __attribute__((ext_vector_type(2), aligned(4)));
          const v2u lo = *(LAS v2a*)(a), hi = *(LAS v2a*)(a + 8);
          kf[k] = __builtin_bit_cast(s8, v4u{lo.x, lo.y, hi.x, hi.y});
          uf[k] = lds_ld<s8>(ua - 64 * r + 512 * k);
        }
#pragma unroll
        for (int k = 0; k < 8; k++)
#pragma unroll
          for (int e = 0; e + k < 8; e++) ya[k + e] = mfma16(kf[k], uf[e], ya[k + e]);
      }
    }
    set_prog(myprog, 6);
    STAMP(6);
    wait_prog(xprog, 6);
    {
      f4 yk[4];
      if (mh == 0) {
#pragma unroll
        for (int j = 0; j < 4; j++) { lds_st<f4>(scr + j * 1024 + lane * 16, ya[4 + j]); yk[j] = ya[j]; }
      } else {
#pragma unroll
        for (int j = 0; j < 4; j++) { lds_st<f4>(scr + j * 1024 + lane * 16, ya[j]); yk[j] = ya[4 + j]; }
      }
      set_prog(myprog, 7);
      wait_one(xprog + (rw ^ 1) * 4, 7);
      const unsigned oscr = lds0 + LD_KT + c * KT_CH + (mh ^ 1) * KT_COPY;
#pragma unroll
      for (int j = 0; j < 4; j++) yk[j] += lds_ld<f4>(oscr + j * 1024 + lane * 16);
#pragma unroll
      for (int z = 0; z < 2; z++) {
        const auto r0 = __builtin_amdgcn_permlane16_swap(pkbf(yk[2 * z][0], yk[2 * z][1]), pkbf(yk[2 * z + 1][0], yk[2 * z + 1][1]), false, false);
        const auto r1 = __builtin_amdgcn_permlane16_swap(pkbf(yk[2 * z][2], yk[2 * z][3]), pkbf(yk[2 * z + 1][2], yk[2 * z + 1][3]), false, false);
        const unsigned e0 = r0[0], e1 = r1[0], e2 = r0[1], e3 = r1[1];
        st128(rs, v4u{e0, e1, e2, e3}, WS_YT + ch * 4096 + (256 * (4 * mh + 2 * z + (g & 1)) + 16 * n16 + 8 * (g >> 1)) * 2, 0);
      }
      WAITV(0);
      if (lane == 0) st32(rs, tag, WS_YFLAG + (((ch >> 6) * 2 + mh) * 64 + (ch & 63)) * 4, 0);
    }
    STAMP(7);
    poll_tag(rs, WS_YFLAG + ((cb * 2 + (tb >> 4)) * 64 + 16 * rw + n16) * 4, tag);
    {
      const int row = 16 * rw + n16;
#pragma unroll
      for (int j = 0; j < 2; j++) {
        const v4u y = ld128(rs, WS_YT + (64 * cb + row) * 4096 + (64 * tb + 32 * j + 8 * g) * 2, 0);
        v4u o;
#pragma unroll
        for (int e = 0; e < 4; e++) o[e] = pkbf(bflo(y[e]) + bflo(xk[j][e]), bfhi(y[e]) + bfhi(xk[j][e]));
        lds_st<v4u>(lds0 + LD_UT + row * 128 + ((j ^ ((row >> 1) & 1)) << 6) + 16 * g, o);
      }
      set_prog(myprog, 8);
    }
    STAMP(8);
  }
}

}

extern "C" void kernel_launch(void* const* d_in, const int* in_sizes, int n_in, void* d_out, int out_size, void* d_ws,
                              size_t ws_size, hipStream_t stream) {
  (void)in_sizes; (void)n_in; (void)out_size; (void)ws_size;
  (void)hipFuncSetAttribute((const void*)fused_kernel, hipFuncAttributeMaxDynamicSharedMemorySize, LDS_BYTES);
  fused_kernel<<<256, 768, LDS_BYTES, stream>>>((const u16*)d_in[0], (const u16*)d_in[1], (const u16*)d_in[2], (const u16*)d_in[3],
                                                (const u16*)d_in[4], (const u16*)d_in[5], (const u16*)d_in[6], (const u16*)d_in[8],
                                                (const u16*)d_in[9], (u16*)d_out, (char*)d_ws);
}
```

```cpp
#include <hip/hip_runtime.h>
#include <stdint.h>

#if defined(__HIP_DEVICE_COMPILE__)
#pragma clang attribute push(__attribute__((target("no-packed-fp32-ops"))), apply_to = function)
#endif
namespace {

typedef unsigned short u16;
typedef unsigned long long u64;
typedef short s4 __attribute__((ext_vector_type(4)));
typedef short s8 __attribute__((ext_vector_type(8)));
typedef short sh2 __attribute__((ext_vector_type(2)));
typedef __bf16 b2 __attribute__((ext_vector_type(2)));
typedef _Float16 h2 __attribute__((ext_vector_type(2)));
typedef _Float16 h8 __attribute__((ext_vector_type(8)));
typedef float f2 __attribute__((ext_vector_type(2)));
typedef float f4 __attribute__((ext_vector_type(4)));
typedef float f16v __attribute__((ext_vector_type(16)));
typedef unsigned v2u __attribute__((ext_vector_type(2)));
typedef unsigned v4u __attribute__((ext_vector_type(4)));
#define LAS __attribute__((address_space(3)))
#define DI __device__ __forceinline__

#define PRIO_L 2
#define PRIO_L2 1
#define PRIO_A 2
#define PRIO_B 1
#define PRIO_X 2
#define PRIO_X2 1
#define PSLEEP 6
#define XGATE 4
#define YGATE 11
constexpr int D_ = 512, F_ = 1792, N_ = 128;
#define CODE_LINES 200

constexpr int WS_EPOCH = 0;
constexpr int WS_YFLAG = 4096;
constexpr int WS_GRAN = 16384;
constexpr int WS_CSUM = 32768;
constexpr int WS_HFLAG = 65536;
constexpr int WS_XT = WS_HFLAG + 2048 * 128;
constexpr int WS_YT = WS_XT + 4 * 1024 * 1024;
constexpr int WS_PART = WS_YT + 2 * 1024 * 1024;
constexpr int WS_H = WS_PART + 4 * 1024 * 1024;
constexpr int WS_END = WS_H + 64 * 112 * 1024;
constexpr int WS_STAMP = 32 * 1024 * 1024;

constexpr int LD_KT = 131072;
constexpr int KT_COPY = 5120 + 64;
constexpr int KT_CH = 2 * KT_COPY;
constexpr int LD_UT = LD_KT + 2 * KT_CH;
constexpr int UT_CH = 4576;
constexpr int LD_CTL = LD_UT + 2 * UT_CH;
constexpr int LD_APROG = LD_CTL, LD_BPROG = LD_CTL + 4, LD_LPROG = LD_CTL + 8, LD_XPROG = LD_CTL + 12, LD_TAG = LD_CTL + 16;
constexpr int LD_RS = LD_CTL + 64;
constexpr int LD_DUMP = LD_RS + 256;
constexpr int LD_END = LD_DUMP + 1024;
constexpr int LDS_BYTES = 163840;
static_assert(LD_END <= LDS_BYTES, "lds");
static_assert(LD_UT % 16 == 0 && LD_CTL % 16 == 0 && KT_COPY % 16 == 0, "align");

constexpr unsigned kLdsSpin = 1u << 18, kMemSpin = 1u << 13;

DI float u2f(unsigned v) { return __builtin_bit_cast(float, v); }
DI unsigned f2u(float v) { return __builtin_bit_cast(unsigned, v); }
DI float bflo(unsigned v) { return u2f(v << 16); }
DI float bfhi(unsigned v) { return u2f(v & 0xffff0000u); }
DI unsigned pkbf(float a, float b) { f2 v = {a, b}; return __builtin_bit_cast(unsigned, __builtin_convertvector(v, b2)); }
DI unsigned pkh(float a, float b) { f2 v = {a, b}; return __builtin_bit_cast(unsigned, __builtin_convertvector(v, h2)); }
DI float rbf(float a) { return bflo(pkbf(a, a)); }
DI float dot2(unsigned a, unsigned b, float acc) {
  return __builtin_amdgcn_fdot2_f32_bf16(__builtin_bit_cast(b2, a), __builtin_bit_cast(b2, b), acc, false);
}
DI unsigned umin(unsigned a, unsigned b) { return a < b ? a : b; }
DI unsigned umax(unsigned a, unsigned b) { return a > b ? a : b; }
DI bool wall(bool p) { return __builtin_amdgcn_ballot_w64(!p) == 0; }
template <class T> DI T lds_ld(unsigned a) { return *(LAS T*)(a); }
template <class T> DI void lds_st(unsigned a, T v) { *(LAS T*)(a) = v; }
DI s4 tr_ld(unsigned a) { return __builtin_amdgcn_ds_read_tr16_b64_v4i16((LAS s4*)(a)); }
DI s8 cat(s4 a, s4 b) { return __builtin_shufflevector(a, b, 0, 1, 2, 3, 4, 5, 6, 7); }
DI f16v mfma32(s8 a, s8 b, f16v c) { return __builtin_amdgcn_mfma_f32_32x32x16_bf16(a, b, c, 0, 0, 0); }
DI f4 mfma16(s8 a, s8 b, f4 c) { return __builtin_amdgcn_mfma_f32_16x16x32_bf16(a, b, c, 0, 0, 0); }
DI f4 mfma16h(v4u a, v4u b, f4 c) {
  return __builtin_amdgcn_mfma_f32_16x16x32_f16(__builtin_bit_cast(h8, a), __builtin_bit_cast(h8, b), c, 0, 0, 0);
}
#define SCHED() __builtin_amdgcn_sched_barrier(0)
#define CFENCE() asm volatile("" ::: "memory")
#define WAITV(n) asm volatile("s_waitcnt vmcnt(" #n ")" ::: "memory")

#define SPIN_SLEEP
DI void set_prog(unsigned addr, unsigned v) { asm volatile("ds_write_b8 %0, %1" ::"v"(addr), "v"(v | 0x80u) : "memory"); }
DI void wait_prog(unsigned addr, unsigned need, unsigned& cache, unsigned mask = 0x80808080u) {
  unsigned t, cnt;
  cache = __builtin_amdgcn_readfirstlane(cache);
  asm volatile(
      "s_mov_b32 %[cnt], %[lim]\n"
      "1:\n\t"
      "s_sub_u32 %[t], %[c], %[n]\n\t"
      "s_andn2_b32 %[t], %[m], %[t]\n\t"
      "s_cbranch_scc0 2f\n\t"
      SPIN_SLEEP
      "ds_read_b32 v127, %[a]\n\t"
      "s_waitcnt lgkmcnt(0)\n\t"
      "v_readfirstlane_b32 %[c], v127\n\t"
      "s_sub_u32 %[cnt], %[cnt], 1\n\t"
      "s_cbranch_scc0 1b\n"
      "2:"
      : [c] "+&s"(cache), [t] "=&s"(t), [cnt] "=&s"(cnt)
      : [a] "v"(addr), [n] "s"(need * 0x01010101u), [m] "s"(mask), [lim] "i"(kLdsSpin)
      : "memory", "scc", "v127");
}
DI bool prog_ok(unsigned cache, unsigned need) { return (0x80808080u & ~(cache - need * 0x01010101u)) == 0; }
DI void glds(unsigned voff, const char* sbase, unsigned m0v) {
  asm volatile("s_mov_b32 m0, %2\n\ts_nop 0\n\tglobal_load_lds_dwordx4 %0, %1" ::"v"(voff), "s"(sbase), "s"(m0v) : "memory");
}
constexpr int SC1 = 16;
constexpr int OOB = 0x7ffffff0;
typedef __amdgpu_buffer_rsrc_t rsrc_t;
DI v4u ld128(rsrc_t r, int voff, int soff) { return __builtin_amdgcn_raw_buffer_load_b128(r, voff, soff, SC1); }
DI v2u ld64(rsrc_t r, int voff, int soff) { return __builtin_amdgcn_raw_buffer_load_b64(r, voff, soff, SC1); }
DI unsigned ld32(rsrc_t r, int voff, int soff) { return __builtin_amdgcn_raw_buffer_load_b32(r, voff, soff, SC1); }
DI void st128(rsrc_t r, v4u v, int voff, int soff) {
  __builtin_amdgcn_raw_buffer_store_b128(v, r, voff, soff, SC1);
  asm volatile("s_nop 1" ::"v"(v) : "memory");
}
DI void st64(rsrc_t r, v2u v, int voff, int soff) { __builtin_amdgcn_raw_buffer_store_b64(v, r, voff, soff, SC1); }
DI void st32(rsrc_t r, unsigned v, int voff, int soff) { __builtin_amdgcn_raw_buffer_store_b32(v, r, voff, soff, SC1); }
DI v4u ldp128(rsrc_t r, int voff, int soff) { return __builtin_amdgcn_raw_buffer_load_b128(r, voff, soff, 0); }
DI void poll_tag(rsrc_t r, int voff, unsigned tag) {
#pragma unroll 1
  for (unsigned i = 0; i < kMemSpin; ++i) {
    CFENCE();
    const unsigned v = ld32(r, voff, 0);
    if (wall(v == tag)) break;
    __builtin_amdgcn_s_sleep(PSLEEP);
  }
  CFENCE();
}
template <int CTRL> DI float dpp(float v) { return u2f(__builtin_amdgcn_update_dpp(0u, f2u(v), CTRL, 0xf, 0xf, true)); }
DI float sum16(float v) { v += dpp<0xB1>(v); v += dpp<0x4E>(v); v += dpp<0x141>(v); return v + dpp<0x140>(v); }
DI float add_x16(float v) { const auto r = __builtin_amdgcn_permlane16_swap(f2u(v), f2u(v), false, false); const unsigned a = r[0], b = r[1]; return u2f(a) + u2f(b); }
DI float add_x32(float v) { const auto r = __builtin_amdgcn_permlane32_swap(f2u(v), f2u(v), false, false); const unsigned a = r[0], b = r[1]; return u2f(a) + u2f(b); }
template <int CTRL> DI unsigned dppmax(unsigned v) { return umax(v, __builtin_amdgcn_update_dpp(0u, v, CTRL, 0xf, 0xf, true)); }
DI unsigned wave_umax(unsigned v) {
  v = dppmax<0x140>(dppmax<0x141>(dppmax<0x4E>(dppmax<0xB1>(v))));
  return umax(umax(__builtin_amdgcn_readlane(v, 0), __builtin_amdgcn_readlane(v, 16)), umax(__builtin_amdgcn_readlane(v, 32), __builtin_amdgcn_readlane(v, 48)));
}
struct cpx { float r, i; };
DI cpx cmul(cpx a, cpx b) { return {a.r * b.r - a.i * b.i, a.r * b.i + a.i * b.r}; }
DI cpx cexp_at(float lr, float rev, float p) {
  const float e = __builtin_amdgcn_exp2f(lr * p);
  const float f = __builtin_amdgcn_fractf(rev * p);
  return {e * __builtin_amdgcn_cosf(f), e * __builtin_amdgcn_sinf(f)};
}
constexpr float kLog2e = 1.44269504088896341f;

#define TR(i)
struct Geo {
  unsigned lds0; int lane, rw, hh, blk, q, pp, g, n16, n32;
};

template <bool IS_A, class F>
DI void gemm1_loop(const Geo& G, f16v (&acc)[2], unsigned& cL, F&& hook) {
  const int th = G.rw >> 1, kh = G.rw & 1;
  const unsigned lprog = G.lds0 + LD_LPROG, myprog = G.lds0 + (IS_A ? LD_APROG : LD_BPROG) + G.rw;
  unsigned ax[4], aw[2];
#pragma unroll
  for (int s = 0; s < 4; s++) ax[s] = G.lds0 + ((IS_A ? 0 : 64) + 32 * th + G.n32) * 128 + (((2 * s + G.hh) ^ ((G.n32 >> 1) & 7)) << 4);
#pragma unroll
  for (int ft = 0; ft < 2; ft++) aw[ft] = G.lds0 + 16384 + (8 * G.hh + G.q) * 256 + (((2 * kh + ft) ^ G.q) << 6) + 32 * G.blk + 8 * G.pp;
  constexpr int PD = 2;
  s8 fx[PD + 1], fw[PD + 1][2];
  auto rdx = [&](int i) { fx[i % (PD + 1)] = lds_ld<s8>(ax[i & 3] + ((i >> 2) & 1) * 32768); };
  auto rdw = [&](int i, int ft) {
    const unsigned a = aw[ft] + ((i >> 2) & 1) * 32768 + (i & 3) * 4096;
    fw[i % (PD + 1)][ft] = cat(tr_ld(a), tr_ld(a + 1024));
  };
  wait_prog(lprog, 1, cL);
  hook(16);
#pragma unroll
  for (int i = 0; i < PD; i++) { rdx(i); rdw(i, 0); rdw(i, 1); }
  unsigned pv = 0;
#pragma unroll
  for (int i = 0; i < 32; i++) {
    const bool p = i + PD < 32;
    SCHED();
    if ((i & 3) == 2) {
      const int s = i >> 2;
      if (p) cL = __builtin_amdgcn_readfirstlane(pv);
      SCHED();
      set_prog(myprog, s + 2);
      hook(s);
      if (p) wait_prog(lprog, (IS_A || s < 3) ? s + 2 : (s < 5 ? 9 : 10), cL);
      hook(s + 8);
      if (p && (s & 1)) {
#pragma unroll
        for (int t = 0; t < 4; t++) ax[t] ^= 65536;
        aw[0] ^= 65536; aw[1] ^= 65536;
      }
      SCHED();
    }
    acc[0] = mfma32(fw[i % (PD + 1)][0], fx[i % (PD + 1)], acc[0]);
    SCHED();
    if ((i & 3) == 0 && i < 28) pv = *(volatile LAS unsigned*)lprog;
    if (p) { rdx(i + PD); rdw(i + PD, 0); }
    SCHED();
    acc[1] = mfma32(fw[i % (PD + 1)][1], fx[i % (PD + 1)], acc[1]);
    SCHED();
    if (p) rdw(i + PD, 1);
  }
  SCHED();
}

__global__ __launch_bounds__(1024, 4) void fused_kernel(
    const u16* __restrict__ x, const u16* __restrict__ logA, const u16* __restrict__ Aim, const u16* __restrict__ Cre,
    const u16* __restrict__ Cim, const u16* __restrict__ logdt, const u16* __restrict__ Dsk, const u16* __restrict__ w1,
    const u16* __restrict__ w2, u16* __restrict__ out, char* ws) {
  extern __shared__ __attribute__((aligned(16))) char smem[];
  u64 pc0;
  asm volatile("s_getpc_b64 %0" : "=s"(pc0));
  const unsigned lds0 = (unsigned)(size_t)(LAS char*)smem;
  const int tid = __builtin_amdgcn_workitem_id_x(), lane = tid & 63;
  const int w = __builtin_amdgcn_readfirstlane(tid >> 6), role = w >> 2, rw = w & 3;
  const int b = __builtin_amdgcn_workgroup_id_x(), xcd = b & 7, idx = b >> 3;
  const int fh = xcd & 1, pl = idx & 3, cb = idx >> 2;
  const bool has1 = cb < 7;
  const int panel = 4 * (xcd >> 1) + pl, slice = 7 * fh + cb;
  const int tb = 2 * panel + fh;
  const rsrc_t rs = __builtin_amdgcn_make_buffer_rsrc(ws, 0, WS_END, 0x00020000);
  const int hh = lane >> 5, blk = (lane >> 4) & 1, q = (lane & 15) >> 2, pp = lane & 3, g = lane >> 4, n16 = lane & 15, n32 = lane & 31;
  const unsigned lprog = lds0 + LD_LPROG, aprog = lds0 + LD_APROG, bprog = lds0 + LD_BPROG, xprog = lds0 + LD_XPROG;
  unsigned cL = 0x80808080u, cA = 0x80808080u, cB = 0x80808080u, cX = 0x80808080u;
  unsigned pA, pI, pCr, pCi, pf;
  v4u i0, i1, i2, i3;
  lds_st<unsigned>(lds0 + LD_CTL + (tid & 3) * 4, 0x80808080u);
  {
    const unsigned po = (unsigned)((2 * b * N_ + 64 * rw + lane) * 2);
    const unsigned co = umin(idx * 8 + (lane & 7), CODE_LINES - 1) * 128;
    const unsigned ro = (unsigned)((64 * tb + 8 * cb + 2 * rw + hh) * 1024 + n32 * 16);
    const unsigned to = (unsigned)((64 * tb + 32 * (rw >> 1) + (lane >> 2)) * 1024 + 128 * cb + 64 * (rw & 1) + (lane & 3) * 16);
    asm volatile(
        "s_cmp_ge_u32 %[role], 2\n\t"
        "s_cbranch_scc1 2f\n\t"
        "s_cmp_eq_u32 %[role], 1\n\t"
        "s_cbranch_scc1 1f\n\t"
        "global_load_ushort %[a0], %[po], %[pA]\n\t"
        "global_load_ushort %[a1], %[po], %[pI]\n\t"
        "global_load_ushort %[a2], %[po], %[pCr]\n\t"
        "global_load_ushort %[a3], %[po], %[pCi]\n\t"
        "global_load_dword %[a4], %[co], %[pc]\n\t"
        "s_branch 2f\n"
        "1:\n\t"
        "global_load_dwordx4 %[i2], %[to], %[x]\n\t"
        "global_load_dwordx4 %[i3], %[t1], %[x]\n\t"
        "global_load_dwordx4 %[i0], %[ro], %[x]\n\t"
        "global_load_dwordx4 %[i1], %[ro], %[x] offset:512\n"
        "2:"
        : [i0] "=&v"(i0), [i1] "=&v"(i1), [i2] "=&v"(i2), [i3] "=&v"(i3), [a0] "=&v"(pA), [a1] "=&v"(pI), [a2] "=&v"(pCr), [a3] "=&v"(pCi),
          [a4] "=&v"(pf)
        : [role] "s"(role), [po] "v"(po), [co] "v"(co), [ro] "v"(ro), [to] "v"(to), [t1] "v"(to + 16384), [x] "s"(x), [pA] "s"(logA), [pI] "s"(Aim),
          [pCr] "s"(Cre), [pCi] "s"(Cim), [pc] "s"(pc0 & ~127ull), "s"(w1), "s"(w2), "s"(out), "s"(ws), "s"(logdt), "s"(Dsk)
        : "memory", "scc");
  }
#define ROLE_END() do { __builtin_amdgcn_endpgm(); __builtin_unreachable(); } while (0)
#define START_BARRIER() asm volatile("s_waitcnt lgkmcnt(0)\n\ts_barrier" ::: "memory")

  if (__builtin_expect(role == 2, 1)) {
    __builtin_amdgcn_s_setprio(PRIO_L);
    const unsigned myprog = lprog + rw;
    if (__builtin_expect(has1, 1)) {
      const int rot1 = (cb + 2 * pl) & 7;
      unsigned xo[2];
#pragma unroll
      for (int j = 0; j < 2; j++) xo[j] = (unsigned)((8 * j + (lane >> 3)) * 1024 + (((lane & 7) ^ ((4 * j + g) & 7)) << 4));
      const unsigned wo = (unsigned)(g * 3584 + ((n16 ^ (g << 2)) << 4));
      const char* xa = (const char*)x + (size_t)((tb ^ 1) * 64 + 16 * rw) * 1024;
      const char* xb = (const char*)x + (size_t)(tb * 64 + 16 * rw) * 1024;
      const char* wb = (const char*)w1 + (size_t)(16 * rw) * 3584 + slice * 256;
      auto issue_x = [&](int s, int grp) {
        const int ss = (s + rot1) & 7;
#pragma unroll
        for (int j = 0; j < 2; j++) glds(xo[j], (grp ? xb : xa) + ss * 128, lds0 + (s & 3) * 32768 + grp * 8192 + rw * 2048 + j * 1024);
      };
      auto issue_w = [&](int s) {
        const int ss = (s + rot1) & 7;
#pragma unroll
        for (int j = 0; j < 4; j++) glds(wo, wb + (size_t)(ss * 64 + 4 * j) * 3584, lds0 + (s & 3) * 32768 + 16384 + rw * 4096 + j * 1024);
      };
      TR(1);
      issue_x(0, 0); issue_w(0); issue_x(0, 1);
      START_BARRIER();
#pragma unroll 1
      for (int s = 1; s < 8; s++) {
        if (s >= 3 && !(prog_ok(cB, s - 2) && (s == 3 || prog_ok(cA, s - 2)))) {
          WAITV(0);
          set_prog(myprog, s);
          wait_prog(bprog, s - 2, cB);
          if (s > 3) wait_prog(aprog, s - 2, cA);
        }
        TR(2 * s);
        issue_x(s, 0); issue_w(s);
        if (s < 4) issue_x(s, 1);
        const v2u pv = *(volatile LAS v2u*)aprog;
        if (s < 4) WAITV(8); else WAITV(6);
        set_prog(myprog, s);
        TR(2 * s + 1);
        cA = __builtin_amdgcn_readfirstlane(pv.x); cB = __builtin_amdgcn_readfirstlane(pv.y);
      }
#pragma unroll
      for (int s = 4; s < 8; s++) issue_x(s, 1);
    } else {
      const int u = (xcd >> 1) * 4 + pl, unit = 14 * fh + umin(u, 13);
      const rsrc_t rw1 = __builtin_amdgcn_make_buffer_rsrc((void*)w1, 0, F_ * D_ * 2, 0x00020000);
      const rsrc_t rw2 = __builtin_amdgcn_make_buffer_rsrc((void*)w2, 0, F_ * D_ * 2, 0x00020000);
      v4u t[16];
      START_BARRIER();
#pragma unroll
      for (int j = 0; j < 16; j++) t[j] = ldp128(rw1, (128 * rw + (lane >> 3)) * 3584 + unit * 128 + (lane & 7) * 16, j * 8 * 3584);
      float a[8];
      unsigned sel_lo = 0x00003f80u, sel_hi = 0x3f800000u;
      asm volatile("" : "+v"(sel_lo), "+v"(sel_hi));
#pragma unroll
      for (int e = 0; e < 8; e++) a[e] = 0.f;
#pragma unroll
      for (int j = 0; j < 16; j++)
#pragma unroll
        for (int e = 0; e < 4; e++) { a[2 * e] = dot2(t[j][e], sel_lo, a[2 * e]); a[2 * e + 1] = dot2(t[j][e], sel_hi, a[2 * e + 1]); }
      lds_st<f4>(lds0 + rw * 2048 + lane * 32, f4{a[0], a[1], a[2], a[3]});
      lds_st<f4>(lds0 + rw * 2048 + lane * 32 + 16, f4{a[4], a[5], a[6], a[7]});
      set_prog(myprog, 1);
      wait_prog(lprog, 1, cL);
      float sm = 0.f;
#pragma unroll
      for (int j = 0; j < 8; j++) sm += lds_ld<float>(lds0 + (8 * g + j) * 256 + (16 * rw + n16) * 4);
      sm = add_x32(add_x16(sm));
      set_prog(myprog, 2);
      wait_prog(bprog, 1, cB);
      const unsigned tag = lds_ld<unsigned>(lds0 + LD_TAG);
      st64(rs, v2u{f2u(sm), tag}, (lane < 16 && u < 14) ? lane * 8 : OOB, WS_CSUM + (unit * 64 + 16 * rw) * 8);
      TR(2);
      wait_prog(xprog, 2, cX);
      unsigned tt[7];
#pragma unroll
      for (int j = 0; j < 7; j++) tt[j] = __builtin_amdgcn_raw_buffer_load_b32(rw2, lane * 128, (896 * fh + 224 * pl + 32 * j + 8 * rw) * 1024, 0);
#pragma unroll
      for (int j = 0; j < 7; j++) asm volatile("" ::"v"(tt[j]));
      wait_prog(lprog, 2, cL);
    }
    {
      const rsrc_t rw2 = __builtin_amdgcn_make_buffer_rsrc((void*)w2, 0, F_ * D_ * 2, 0x00020000);
      const int wo2 = (896 * fh + 32 * rw + (lane >> 3)) * 1024 + cb * 128 + (lane & 7) * 16;
      v4u wq[7][4];
      auto ldw2 = [&](int u) {
        int sg = u + 2 * pl; sg = sg >= 7 ? sg - 7 : sg;
#pragma unroll
        for (int j = 0; j < 4; j++) wq[u][j] = ldp128(rw2, wo2, sg * 131072 + j * 8192);
        CFENCE();
      };
      if (has1) { WAITV(8); set_prog(myprog, 8); }
      TR(16);
      ldw2(0);
      if (has1) { WAITV(8); set_prog(myprog, 9); }
      TR(17);
      ldw2(1);
      if (has1) { WAITV(8); set_prog(myprog, 10); } else set_prog(myprog, 10);
      TR(18);
      __builtin_amdgcn_s_setprio(PRIO_L2);
#pragma unroll
      for (int u = 2; u < 7; u++) ldw2(u);
      set_prog(myprog, 11);
      TR(19);
      const unsigned wdst = lds0 + rw * 4096 + (lane >> 3) * 128 + (((lane & 7) ^ (((lane >> 4) & 1) << 2)) << 4);
#pragma unroll
      for (int u = 0; u < 7; u++) {
        if (!(u & 1)) { wait_prog(bprog, 6 + (u >> 1), cB); wait_prog(aprog, 6 + (u >> 1), cA); TR(20 + (u >> 1)); }
#pragma unroll
        for (int j = 0; j < 4; j++) lds_st<v4u>(wdst + u * 16384 + j * 1024, wq[u][j]);
      }
      set_prog(myprog, 15);
    }
    ROLE_END();
  } else if (role == 0) {
    __builtin_amdgcn_s_setprio(PRIO_X);
    const unsigned myprog = xprog + rw, pmask = 0x80u << (8 * (rw ^ 1));
    const int cp = b, c = rw >> 1, mh = rw & 1, ch = 2 * cp + c;
    START_BARRIER();
    unsigned sdt, sD;
    asm volatile("s_load_dword %0, %2, %4\n\ts_load_dword %1, %3, %4\n\ts_waitcnt lgkmcnt(0)"
                 : "=&s"(sdt), "=&s"(sD) : "s"(logdt), "s"(Dsk), "s"(b * 4) : "memory");
    const unsigned pdt = c ? sdt >> 16 : sdt & 0xffffu, pD = c ? sD >> 16 : sD & 0xffffu;
    asm volatile("s_waitcnt vmcnt(1)" : "+v"(pA), "+v"(pI), "+v"(pCr), "+v"(pCi), "+v"(pf)::"memory");
    const unsigned scr = lds0 + LD_KT + c * KT_CH + mh * KT_COPY;
    const unsigned oscr = lds0 + LD_KT + c * KT_CH + (mh ^ 1) * KT_COPY;
    f4 kd[3];
    {
      const float dt = rbf(__builtin_amdgcn_exp2f(bflo(pdt) * kLog2e));
      const float lr = -rbf(__builtin_amdgcn_exp2f(bflo(pA) * kLog2e)) * dt * kLog2e;
      const float rev = bflo(pI) * dt * 0.15915494309189535f;
      TR(1);
      cpx C = {bflo(pCr) * dt, bflo(pCi) * dt};
      const unsigned eb = umin(umax(wave_umax(umax(f2u(C.r) & 0x7fffffffu, f2u(C.i) & 0x7fffffffu)) >> 23, 12u), 254u);
      const float sdn = u2f((266u - eb) << 23), sup = u2f((eb - 11u) << 23);
      C.r *= sdn; C.i *= -sdn;
      const cpx e8 = cexp_at(lr, rev, 8.f);
      const cpx e1 = cexp_at(lr, -rev, 1.f), e2 = cmul(e1, e1), e4 = cmul(e2, e2), gg = cexp_at(lr, -rev, 128.f);
      const unsigned wa = scr + lane * 4, ra = scr + n16 * 272 + g * 16;
      auto put = [&](int i, cpx v) { lds_st<unsigned>(wa + i * 272, pkh(v.r, v.i)); };
      v4u fq[4];
      {
        const cpx e16 = cmul(e8, e8);
        cpx a = e16, bq = cmul(e8, e16);
        put(0, cpx{1.f, 0.f}); put(1, e8);
#pragma unroll
        for (int j = 2; j < 16; j += 2) {
          put(j, a); put(j + 1, bq);
          if (j < 14) { a = cmul(a, e16); bq = cmul(bq, e16); }
        }
        CFENCE();
#pragma unroll
        for (int s = 0; s < 4; s++) fq[s] = lds_ld<v4u>(ra + s * 64);
        CFENCE();
      }
      auto tile = [&](int T) {
        CFENCE();
        v4u fp[4];
#pragma unroll
        for (int s = 0; s < 4; s++) fp[s] = lds_ld<v4u>(ra + s * 64);
        CFENCE();
        f4 a = {0.f, 0.f, 0.f, 0.f};
#pragma unroll
        for (int s = 0; s < 4; s++) a = mfma16h(fp[s], fq[s], a);
        kd[T] = a * sup;
      };
      cpx P = C;
      {
        cpx a = P, bq = cmul(P, gg);
        P = cmul(bq, gg);
#pragma unroll
        for (int j = 0; j < 8; j++) {
          put(j, a); put(8 + j, bq);
          if (j < 7) { a = cmul(a, e1); bq = cmul(bq, e1); }
        }
      }
      tile(0);
      const cpx P2 = P, P3 = cmul(P, gg), P4 = cmul(P3, gg);
      P = P4;
#pragma unroll
      for (int j = 0; j < 12; j++) {
        put(4 * (j & 3) + (j < 4 ? 0 : 1 + (j >> 2)), P);
        if (j < 4) put(4 * j + 1, cmul(P, e4));
        P = cmul(P, gg);
      }
      tile(1);
      {
        cpx a = P2, bq = P3;
#pragma unroll
        for (int j = 0; j < 4; j++) {
          put(j, a); put(4 + j, bq);
          if (j < 3) { a = cmul(a, e2); bq = cmul(bq, e2); }
        }
        put(8, P4); put(9, P);
      }
      tile(2);
    }
    asm volatile("s_waitcnt vmcnt(0)" : "+v"(pf)::"memory");
    TR(2);
    wait_prog(lprog, has1 ? XGATE : 2, cL);
    const int tok0 = 512 * rw + 8 * lane;
    v4u gq[4], xv[4];
    auto gather = [&]() {
#pragma unroll
      for (int j = 0; j < 4; j++) gq[j] = ld128(rs, WS_GRAN + lane * 16, (rw * 4 + j) * 1024);
#pragma unroll
      for (int j = 0; j < 4; j++) xv[j] = ld128(rs, (tok0 >> 5) * 128 + (lane & 3) * 16 + (j >> 1) * 64, WS_XT + cp * 16384 + (j & 1) * 8192);
      CFENCE();
    };
    TR(10);
    gather();
    TR(11);
    const unsigned ut = lds0 + LD_UT + c * UT_CH, kt = lds0 + LD_KT + c * KT_CH;
    if (mh == 0) {
      lds_st<f4>(scr + 1024 + lane * 16, kd[1]);
      lds_st<float>(scr + 2048 + lane * 4, kd[2][1]);
      lds_st<v4u>(ut + umin(lane, 29) * 16, v4u{0u, 0u, 0u, 0u});
      set_prog(myprog, 2);
      wait_prog(xprog, 2, cX, pmask);
      f4 v = kd[0] + lds_ld<f4>(oscr + 3072 + lane * 16);
      const f4 u = kd[2] + lds_ld<f4>(oscr + 4096 + lane * 16);
      CFENCE();
      v[0] += lane == 0 ? bflo(pD) : 0.f;
      const float nx = u2f(__builtin_amdgcn_update_dpp(0u, f2u(u[0]), 0x130, 0xf, 0xf, false));
      const unsigned a1 = lane < 32 ? kt + 3568 - 16 * lane : lds0 + LD_DUMP;
      const unsigned u22 = pkbf(u[2], u[2]), u00 = pkbf(u[0], u[0]);
      lds_st<v4u>(a1, v4u{pkbf(nx, u[3]), u22, pkbf(u[2], u[1]), u00});
      lds_st<v4u>(lane < 32 ? a1 + KT_COPY : a1, v4u{pkbf(u[3], u[2]), u22, pkbf(u[1], u[0]), u00});
      CFENCE();
      const unsigned a0 = kt + 4088 - 2 * (128 * (g >> 1) + 8 * n16 + 4 * (g & 1));
      const unsigned v32 = pkbf(v[3], v[2]), v10 = pkbf(v[1], v[0]);
      lds_st<v2u>(a0, v2u{v32, v10});
      lds_st<u16>(a0 + KT_COPY - 2, (u16)v32);
      lds_st<unsigned>(a0 + KT_COPY, pkbf(v[2], v[1]));
      lds_st<u16>(a0 + KT_COPY + 4, (u16)(v10 >> 16));
      lds_st<u16>(kt + KT_COPY + 4094, (u16)0);
      lds_st<unsigned>(kt + 4096 + (lane & 7) * 4 + ((lane >> 3) & 1) * KT_COPY, 0u);
    } else {
      lds_st<f4>(scr + 3072 + lane * 16, kd[0]);
      lds_st<f4>(scr + 4096 + lane * 16, kd[2]);
      set_prog(myprog, 2);
      wait_prog(xprog, 2, cX, pmask);
      const f4 v = kd[1] + lds_ld<f4>(oscr + 1024 + lane * 16);
      const float kend = u2f(__builtin_amdgcn_readlane(f2u(kd[2][1]), 32)) + lds_ld<float>(oscr + 2048 + 128);
      CFENCE();
      auto next = [&](float cur, unsigned last) { return u2f(__builtin_amdgcn_update_dpp(last, f2u(cur), 0x130, 0xf, 0xf, false)); };
      auto rol = [&](float t) { return __builtin_amdgcn_update_dpp(0u, f2u(t), 0x134, 0xf, 0xf, false); };
      const float n0 = next(v[0], rol(v[2])), n2 = next(v[2], rol(v[3])), n3 = next(v[3], f2u(kend));
      const unsigned a1 = kt + 1008 - 16 * lane;
      {
        const unsigned v11 = pkbf(v[1], v[1]), v00 = pkbf(v[0], v[0]);
        lds_st<v4u>(a1 + 2048, v4u{pkbf(n0, n0), v11, pkbf(v[1], v[0]), v00});
        lds_st<v4u>(a1 + 2048 + KT_COPY, v4u{pkbf(n0, v[1]), v11, v00, v00});
      }
#pragma unroll
      for (int r = 2; r < 4; r++) {
        const float nx = r == 2 ? n2 : n3, z = (lane & 1) ? nx : v[r];
        const unsigned nn = pkbf(nx, nx), vv = pkbf(v[r], v[r]);
        lds_st<v4u>(a1 + (3 - r) * 1024, v4u{nn, pkbf(nx, z), vv, vv});
        lds_st<v4u>(a1 + (3 - r) * 1024 + KT_COPY, v4u{nn, pkbf(z, v[r]), vv, vv});
      }
    }
    TR(3);
    wait_prog(bprog, 1, cB);
    const unsigned tag = lds_ld<unsigned>(lds0 + LD_TAG);
    auto gathered = [&]() {
      unsigned bad = 0;
#pragma unroll
      for (int j = 0; j < 4; j++) bad |= (gq[j].y ^ tag) | (gq[j].w ^ tag) | (xv[j].y ^ tag) | (xv[j].w ^ tag);
      return wall(bad == 0);
    };
    if (!gathered()) {
#pragma unroll 1
      for (unsigned i = 0; i < kMemSpin; ++i) {
        __builtin_amdgcn_s_sleep(8);
        TR(12);
        gather();
        if (gathered()) break;
      }
    }
    TR(4);
    {
      float mr[8], ri[8];
#pragma unroll
      for (int j = 0; j < 4; j++) {
        ri[2 * j] = __builtin_amdgcn_rcpf(bfhi(gq[j].x)); mr[2 * j] = -bflo(gq[j].x) * ri[2 * j];
        ri[2 * j + 1] = __builtin_amdgcn_rcpf(bfhi(gq[j].z)); mr[2 * j + 1] = -bflo(gq[j].z) * ri[2 * j + 1];
      }
#pragma unroll
      for (int k = 0; k < 2; k++) {
        v4u o;
#pragma unroll
        for (int j = 0; j < 4; j++) {
          const unsigned d = xv[2 * k + (j >> 1)][2 * (j & 1)];
          o[j] = pkbf(bflo(d) * ri[2 * j] + mr[2 * j], bfhi(d) * ri[2 * j + 1] + mr[2 * j + 1]);
        }
        lds_st<v4u>(lds0 + LD_UT + k * UT_CH + 480 + tok0 * 2, o);
      }
      set_prog(myprog, 5);
      if ((tb >> 3) == rw) {
        const unsigned ar = lds0 + ((tok0 >> 6) == tb ? LD_RS : LD_DUMP) + (tok0 & 63) * 4;
        lds_st<f4>(ar, f4{ri[0], ri[1], ri[2], ri[3]});
        lds_st<f4>(ar + 16, f4{ri[4], ri[5], ri[6], ri[7]});
      }
      wait_prog(xprog, 5, cX);
      if (b == 0 && rw == 0) st32(rs, tag, lane == 0 ? 0 : OOB, WS_EPOCH);
    }
    TR(5);
    __builtin_amdgcn_s_setprio(PRIO_X2);
    f4 ya[8];
#pragma unroll
    for (int i = 0; i < 8; i++) ya[i] = f4{0.f, 0.f, 0.f, 0.f};
    {
      const unsigned ka = lds0 + LD_KT + c * KT_CH + (1 - (n16 & 1)) * KT_COPY + 4 * (1015 - (n16 >> 1) + 4 * g) - 64 * (59 + 4 * mh);
      const unsigned ua = ut + 2 * (224 + 16 * n16 + 8 * g) - 64 * (3 + 4 * mh);
      unsigned kb[4];
#pragma unroll
      for (int j = 0; j < 4; j++) { kb[j] = ka + 1024 * j; asm volatile("" : "+v"(kb[j])); }
      typedef unsigned v2a __attribute__((ext_vector_type(2), aligned(4)));
      v2u kl[2][8], kh_[2][8];
      s8 uf[2][8];
#define KL(r, k) kl[(r) & 1][k] = *(LAS v2a*)(kb[(7 - (k)) >> 1] + 64 * (3 - (r)) + 512 * ((7 - (k)) & 1))
#define KH(r, k) kh_[(r) & 1][k] = *(LAS v2a*)(kb[(7 - (k)) >> 1] + 64 * (3 - (r)) + 512 * ((7 - (k)) & 1) + 8)
#define UL(r, e) uf[(r) & 1][e] = lds_ld<s8>(ua + 64 * (3 - (r)) + 512 * (e))
#define MM(r, k, e) SCHED(); ya[(k) + (e)] = mfma16(__builtin_bit_cast(s8, v4u{kl[(r) & 1][k].x, kl[(r) & 1][k].y, kh_[(r) & 1][k].x, kh_[(r) & 1][k].y}), uf[(r) & 1][e], ya[(k) + (e)]); SCHED()
      KL(0, 0); KH(0, 0); UL(0, 7); UL(0, 6); UL(0, 5); UL(0, 4); UL(0, 3); UL(0, 2); UL(0, 1); UL(0, 0); KL(0, 1); KH(0, 1);
      MM(0, 0, 7); MM(0, 0, 6); MM(0, 0, 5); MM(0, 0, 4); MM(0, 0, 3); KH(0, 2); MM(0, 0, 2); KL(0, 2); MM(0, 0, 1); MM(0, 0, 0);
      MM(0, 1, 6); MM(0, 1, 5); MM(0, 1, 4); KH(0, 3); MM(0, 1, 3); KL(0, 3); MM(0, 1, 2); MM(0, 1, 1); MM(0, 1, 0);
      MM(0, 2, 5); KH(0, 4); MM(0, 2, 4); KL(0, 4); MM(0, 2, 3); KH(0, 5); MM(0, 2, 2); KL(0, 5); MM(0, 2, 1); KH(0, 6);
      MM(0, 2, 0); KL(0, 6); MM(0, 3, 4); KH(0, 7); MM(0, 3, 3); KL(0, 7); MM(0, 3, 2); KH(1, 0); MM(0, 3, 1); KL(1, 0);
      MM(0, 3, 0); UL(1, 7); MM(0, 4, 3); UL(1, 6); MM(0, 4, 2); KH(1, 1); MM(0, 4, 1); KL(1, 1); MM(0, 4, 0); UL(1, 5);
      MM(0, 5, 2); KH(1, 2); MM(0, 5, 1); KL(1, 2); MM(0, 5, 0); UL(1, 4); MM(0, 6, 1); MM(0, 6, 0); KH(1, 3); MM(0, 7, 0); KL(1, 3);
      MM(1, 0, 7); UL(1, 3); MM(1, 0, 6); MM(1, 1, 6); MM(1, 0, 5); KH(1, 4); MM(1, 1, 5); KL(1, 4); MM(1, 2, 5); UL(1, 2); MM(1, 0, 4);
      MM(1, 1, 4); MM(1, 2, 4); MM(1, 3, 4); KH(1, 5); MM(1, 0, 3); KL(1, 5); MM(1, 1, 3); UL(1, 1); MM(1, 2, 3); MM(1, 3, 3);
      MM(1, 4, 3); MM(1, 0, 2); MM(1, 1, 2); KH(1, 6); MM(1, 2, 2); KL(1, 6); MM(1, 3, 2); UL(1, 0); MM(1, 4, 2); MM(1, 5, 2);
      MM(1, 0, 1); KH(1, 7); MM(1, 1, 1); KL(1, 7); MM(1, 2, 1); KH(2, 0); MM(1, 3, 1); KL(2, 0); MM(1, 4, 1); UL(2, 7);
      MM(1, 5, 1); UL(2, 6); MM(1, 6, 1); UL(2, 5); MM(1, 0, 0); UL(2, 4); MM(1, 1, 0); UL(2, 3); MM(1, 2, 0); UL(2, 2);
      MM(1, 3, 0); UL(2, 1); MM(1, 4, 0); UL(2, 0); MM(1, 5, 0); KH(2, 1); MM(1, 6, 0); KL(2, 1); MM(1, 7, 0); MM(2, 0, 7); MM(2, 0, 6);
      MM(2, 0, 5); MM(2, 0, 4); MM(2, 0, 3); KH(2, 2); MM(2, 0, 2); KL(2, 2); MM(2, 0, 1); MM(2, 0, 0); MM(2, 1, 6); MM(2, 1, 5);
      MM(2, 1, 4); KH(2, 3); MM(2, 1, 3); KL(2, 3); MM(2, 1, 2); MM(2, 1, 1); MM(2, 1, 0); MM(2, 2, 5); KH(2, 4); MM(2, 2, 4); KL(2, 4);
      MM(2, 2, 3); KH(2, 5); MM(2, 2, 2); KL(2, 5); MM(2, 2, 1); KH(2, 6); MM(2, 2, 0); KL(2, 6); MM(2, 3, 4); KH(2, 7);
      MM(2, 3, 3); KL(2, 7); MM(2, 3, 2); KH(3, 0); MM(2, 3, 1); KL(3, 0); MM(2, 3, 0); UL(3, 7); MM(2, 4, 3); UL(3, 6);
      MM(2, 4, 2); KH(3, 1); MM(2, 4, 1); KL(3, 1); MM(2, 4, 0); UL(3, 5); MM(2, 5, 2); KH(3, 2); MM(2, 5, 1); KL(3, 2);
      MM(2, 5, 0); UL(3, 4); MM(2, 6, 1); MM(2, 6, 0); KH(3, 3); MM(2, 7, 0); KL(3, 3); MM(3, 0, 7); UL(3, 3); MM(3, 0, 6); MM(3, 1, 6);
      MM(3, 0, 5); KH(3, 4); MM(3, 1, 5); KL(3, 4); MM(3, 2, 5); UL(3, 2); MM(3, 0, 4); MM(3, 1, 4); MM(3, 2, 4); MM(3, 3, 4); KH(3, 5);
      MM(3, 0, 3); KL(3, 5); MM(3, 1, 3); UL(3, 1); MM(3, 2, 3); MM(3, 3, 3); MM(3, 4, 3); MM(3, 0, 2); MM(3, 1, 2); KH(3, 6);
      MM(3, 2, 2); KL(3, 6); MM(3, 3, 2); UL(3, 0); MM(3, 4, 2); MM(3, 5, 2); MM(3, 0, 1); MM(3, 1, 1); MM(3, 2, 1);
      MM(3, 3, 1); KH(3, 7); MM(3, 4, 1); KL(3, 7); set_prog(myprog, 6); MM(3, 5, 1); MM(3, 6, 1); MM(3, 0, 0); MM(3, 1, 0);
      MM(3, 2, 0); MM(3, 3, 0); MM(3, 4, 0); MM(3, 5, 0); MM(3, 6, 0); MM(3, 7, 0);
#undef KL
#undef KH
#undef UL
#undef MM
      SCHED();
    }
    __builtin_amdgcn_s_setprio(3);
    TR(6);
    wait_prog(xprog, 6, cX);
    {
      f4 yk[4];
      if (mh == 0) {
#pragma unroll
        for (int j = 0; j < 4; j++) { lds_st<f4>(scr + j * 1024 + lane * 16, ya[4 + j]); yk[j] = ya[j]; }
      } else {
#pragma unroll
        for (int j = 0; j < 4; j++) { lds_st<f4>(scr + j * 1024 + lane * 16, ya[j]); yk[j] = ya[4 + j]; }
      }
      set_prog(myprog, 7);
      wait_prog(xprog, 7, cX, pmask);
#pragma unroll
      for (int j = 0; j < 4; j++) yk[j] += lds_ld<f4>(oscr + j * 1024 + lane * 16);
      set_prog(myprog, 8);
#pragma unroll
      for (int z = 0; z < 2; z++) {
        const auto r0 = __builtin_amdgcn_permlane16_swap(pkbf(yk[2 * z][0], yk[2 * z][1]), pkbf(yk[2 * z + 1][0], yk[2 * z + 1][1]), false, false);
        const auto r1 = __builtin_amdgcn_permlane16_swap(pkbf(yk[2 * z][2], yk[2 * z][3]), pkbf(yk[2 * z + 1][2], yk[2 * z + 1][3]), false, false);
        const unsigned e0 = r0[0], e1 = r1[0], e2 = r0[1], e3 = r1[1];
        st128(rs, v4u{e0, e1, e2, e3}, WS_YT + (256 * (2 * z + (g & 1)) + 16 * n16 + 8 * (g >> 1)) * 2, ch * 4096 + mh * 2048);
      }
      TR(13);
      WAITV(0);
      st32(rs, tag, lane == 0 ? 0 : OOB, WS_YFLAG + (((ch >> 6) * 2 + mh) * 64 + (ch & 63)) * 4);
    }
    TR(7);
    wait_prog(bprog, YGATE, cB);
    TR(14);
    {
      const int yo = WS_YFLAG + ((cb * 2 + (tb >> 4)) * 64 + 16 * rw + n16) * 4;
      if (!wall(ld32(rs, yo, 0) == tag)) { wait_prog(bprog, 13, cB); poll_tag(rs, yo, tag); }
      CFENCE();
    }
    TR(15);
    {
#pragma unroll
      for (int j = 0; j < 2; j++) {
        const int row = 16 * rw + 8 * j + (lane >> 3);
        const v4u y = ld128(rs, WS_YT + row * 4096 + (lane & 7) * 16, cb * 64 * 4096 + tb * 128);
        lds_st<v4u>(lds0 + LD_UT + row * 128 + (((lane & 7) ^ (((lane >> 4) & 1) << 2)) << 4), y);
      }
      set_prog(myprog, 9);
    }
    ROLE_END();
  } else {
    const bool isA = role == 3;
    if (isA) __builtin_amdgcn_s_setprio(PRIO_A); else __builtin_amdgcn_s_setprio(3);
    const unsigned gprog = isA ? aprog : bprog, myprog = gprog + rw, pmask = 0x80u << (8 * (rw ^ 1));
    const int th = rw >> 1, kh = rw & 1;
    const int tbk = isA ? tb ^ 1 : tb;
    unsigned cG = 0x80808080u, tag = 0;
    v2u xres[4];
#pragma unroll
    for (int j = 0; j < 4; j++) xres[j] = v2u{0u, 0u};
    if (isA) {
      START_BARRIER();
      if (!has1) set_prog(myprog, 9);
    } else {
      const int st0 = 64 * tb + 8 * cb + 2 * rw;
      START_BARRIER();
      unsigned tg;
      asm volatile("s_load_dword %0, %1, 0x0\n\ts_waitcnt lgkmcnt(0)" : "=s"(tg) : "s"(ws) : "memory");
      tg += 1u;
      tag = tg;
      lds_st<unsigned>(lds0 + LD_TAG, tg);
      asm volatile("s_waitcnt vmcnt(0)" : "+v"(i0), "+v"(i1), "+v"(i2), "+v"(i3)::"memory");
      const v4u r0 = i0, r1 = i1, t0 = i2, t1 = i3;
      const unsigned scr = lds0 + 7 * 16384 + rw * 2048;
      lds_st<v4u>(scr + lane * 16, t0);
      lds_st<v4u>(scr + 1024 + lane * 16, t1);
      CFENCE();
      v4u tr[2];
#pragma unroll
      for (int j = 0; j < 2; j++) {
        const unsigned a = scr + (8 * g + q) * 64 + 32 * j + 8 * pp;
        tr[j] = __builtin_bit_cast(v4u, cat(tr_ld(a), tr_ld(a + 256)));
      }
#pragma unroll
      for (int j = 0; j < 4; j++) xres[j] = lds_ld<v2u>(scr + n32 * 64 + 16 * j + 8 * hh);
      set_prog(myprog, has1 ? 1 : 9);
      TR(1);
#pragma unroll
      for (int j = 0; j < 2; j++) {
        const int cho = 64 * cb + 32 * kh + 16 * j + n16;
#pragma unroll
        for (int hf2 = 0; hf2 < 2; hf2++)
          st128(rs, v4u{tr[j][2 * hf2], tg, tr[j][2 * hf2 + 1], tg}, (cho >> 1) * 16384 + (cho & 1) * 64 + g * 16, WS_XT + hf2 * 8192 + (2 * tb + th) * 128);
      }
      float s1 = 0.f, s2 = 0.f;
#pragma unroll
      for (int j = 0; j < 4; j++) {
        s1 = dot2(r1[j], 0x3f803f80u, dot2(r0[j], 0x3f803f80u, s1));
        s2 = dot2(r1[j], r1[j], dot2(r0[j], r0[j], s2));
      }
      const float m = add_x16(sum16(s1)) * (1.f / D_);
      const float var = rbf(__builtin_fmaxf(add_x16(sum16(s2)) * (1.f / D_) - m * m, 0.f));
      const unsigned gv = pkbf(m, __builtin_amdgcn_sqrtf(rbf(var + 1e-5f)));
      const int sbk = st0 >> 3;
      st64(rs, v2u{gv, tg}, n32 == 0 ? hh * 8 : OOB, WS_GRAN + (((sbk >> 6) * 4 + rw) * 64 + (sbk & 63)) * 16);
      __builtin_amdgcn_s_setprio(PRIO_A);
    }
    TR(2);
    constexpr int WIN = 3;
    v4u hf[WIN][4];
    const int hb = WS_H + ((2 * tbk + th) * 112 + 56 * fh + 4 * kh) * 1024;
    const int hfl = WS_HFLAG + ((tbk * 2 + fh) * 4 + rw) * 1024;
    if (has1) {
      f16v acc[2];
#pragma unroll
      for (int i = 0; i < 16; i++) acc[0][i] = acc[1][i] = 0.f;
      const Geo G = {lds0, lane, rw, hh, blk, q, pp, g, n16, n32};
      const int t = 64 * tbk + 32 * th + n32;
      const int mo = WS_GRAN + ((((t >> 9) * 4 + ((t >> 1) & 3)) * 64 + ((t >> 3) & 63)) * 2 + (t & 1)) * 8;
      const int co = WS_CSUM + (slice * 128 + 64 * kh + lane) * 8;
      v2u gm, gc;
      if (isA) {
        gemm1_loop<true>(G, acc, cL, [&](int s) { if (s >= 8) TR(s == 16 ? 3 : s - 4); if (s == 5) { gm = ld64(rs, mo, 0); gc = ld64(rs, co, 0); tag = lds_ld<unsigned>(lds0 + LD_TAG); } });
      } else {
        gemm1_loop<false>(G, acc, cL, [&](int s) {
          if (s >= 8) TR(s == 16 ? 3 : s - 4);
          if (s == 3) { gm = ld64(rs, mo, 0); gc = ld64(rs, co, 0); __builtin_amdgcn_s_setprio(PRIO_B); }
        });
      }

      if (!wall(gm.y == tag && gc.y == tag)) {
#pragma unroll 1
        for (unsigned i = 0; i < kMemSpin; ++i) {
          CFENCE();
          gm = ld64(rs, mo, 0); gc = ld64(rs, co, 0);
          if (wall(gm.y == tag && gc.y == tag)) break;
          __builtin_amdgcn_s_sleep(4);
        }
      }
      {
        const unsigned m = (gm.x ^ 0x8000u) & 0xffffu;
        const s8 cm = __builtin_bit_cast(s8, v4u{m | (m << 16), m, 0u, 0u});
        const float cs = u2f(gc.x), c0 = rbf(cs), c1 = rbf(cs - c0);
        const unsigned p0 = pkbf(c0, c1), p1 = pkbf(cs - c0 - c1, 0.f);
        acc[0] = mfma32(__builtin_bit_cast(s8, v4u{hh ? 0u : p0, hh ? 0u : p1, 0u, 0u}), cm, acc[0]);
        acc[1] = mfma32(__builtin_bit_cast(s8, v4u{hh ? p0 : 0u, hh ? p1 : 0u, 0u, 0u}), cm, acc[1]);
      }
      TR(12);
#pragma unroll
      for (int s = 0; s < 4; s++) {
#pragma unroll
        for (int j = 0; j < 4; j++) {
          const unsigned p = pkbf(acc[s >> 1][8 * (s & 1) + 2 * j], acc[s >> 1][8 * (s & 1) + 2 * j + 1]);
          hf[0][s][j] = __builtin_bit_cast(unsigned, __builtin_elementwise_max(__builtin_bit_cast(sh2, p), sh2{0, 0}));
        }
        st128(rs, hf[0][s], lane * 16 + s * 1024, hb + cb * 8192);
      } TR(13);
      WAITV(0);
      TR(14);
      st32(rs, tag, lane == 0 ? 0 : OOB, hfl + cb * 128);
    } else if (isA) {
      wait_prog(bprog, 1, cB);
      tag = lds_ld<unsigned>(lds0 + LD_TAG);
    }
    wait_prog(lprog, 11, cL);
    if (!isA) { __builtin_amdgcn_s_setprio(PRIO_B); wait_prog(aprog, 12, cA); set_prog(myprog, 11); }
    TR(15);
    poll_tag(rs, hfl + umin(lane, 6) * 128, tag);
    TR(16);
    TR(17);
    auto ldh = [&](int c, int s) {
      int sg = c + cb; sg = sg >= 7 ? sg - 7 : sg;
      hf[c % WIN][s] = ld128(rs, lane * 16 + s * 1024, hb + sg * 8192);
    };
    if (!has1) {
#pragma unroll
      for (int s = 0; s < 4; s++) ldh(0, s);
    }
#pragma unroll
    for (int c = 1; c < WIN; c++)
#pragma unroll
      for (int s = 0; s < 4; s++) { ldh(c, s); SCHED(); }
    set_prog(myprog, 12);
    unsigned a2[2];
    int un = cb + 7 - 2 * pl; un = un >= 7 ? un - 7 : un; un = un >= 7 ? un - 7 : un;
#pragma unroll
    for (int ct = 0; ct < 2; ct++) a2[ct] = lds0 + un * 16384 + (64 * kh + 4 * hh + q) * 128 + ((ct ^ (q >> 1)) << 6) + 32 * blk + 8 * pp;
    constexpr int PD2 = 2;
    s8 fw2[PD2 + 1][2];
    auto rd2 = [&](int i) {
      if (i > 0 && (i & 3) == 0) {
        const int d = un == 6 ? -6 * 16384 : 16384;
        un = un == 6 ? 0 : un + 1;
        a2[0] += d; a2[1] += d;
      }
#pragma unroll
      for (int ct = 0; ct < 2; ct++) {
        const unsigned a = a2[ct] + (i & 3) * 2048;
        fw2[i % (PD2 + 1)][ct] = cat(tr_ld(a), tr_ld(a + 1024));
      }
    };
    wait_prog(lprog, 15, cL);
#pragma unroll
    for (int i = 0; i < PD2; i++) rd2(i);
    f16v ac2[2];
#pragma unroll
    for (int i = 0; i < 16; i++) ac2[0][i] = ac2[1][i] = 0.f;
#pragma unroll
    for (int i = 0; i < 28; i++) {
      const int c = i >> 2, s = i & 3;
      SCHED();
      ac2[0] = mfma32(fw2[i % (PD2 + 1)][0], __builtin_bit_cast(s8, hf[c % WIN][s]), ac2[0]);
      ac2[1] = mfma32(fw2[i % (PD2 + 1)][1], __builtin_bit_cast(s8, hf[c % WIN][s]), ac2[1]);
      SCHED();
      if (i + PD2 < 28) rd2(i + PD2);
      if (c + WIN < 7) ldh(c + WIN, s);
      if (s == 3) TR(18 + c);
      if (i == 15) set_prog(myprog, 13);
    }
    SCHED();
    if (!isA) wait_prog(xprog, 8, cX);
    const unsigned ex = isA ? lds0 + 7 * 16384 : lds0 + LD_KT;
    f16v keep;
    if (kh == 0) {
#pragma unroll
      for (int j = 0; j < 4; j++) lds_st<f4>(ex + rw * 4096 + j * 1024 + lane * 16, f4{ac2[1][4 * j], ac2[1][4 * j + 1], ac2[1][4 * j + 2], ac2[1][4 * j + 3]});
      keep = ac2[0];
    } else {
#pragma unroll
      for (int j = 0; j < 4; j++) lds_st<f4>(ex + rw * 4096 + j * 1024 + lane * 16, f4{ac2[0][4 * j], ac2[0][4 * j + 1], ac2[0][4 * j + 2], ac2[0][4 * j + 3]});
      keep = ac2[1];
    }
    set_prog(myprog, 14);
    TR(25);
    wait_prog(gprog, 14, cG, pmask);
#pragma unroll
    for (int j = 0; j < 4; j++) {
      const f4 o = lds_ld<f4>(ex + (rw ^ 1) * 4096 + j * 1024 + lane * 16);
#pragma unroll
      for (int e = 0; e < 4; e++) keep[4 * j + e] += o[e];
    }
    const unsigned bx = lds0 + 7 * 16384 + (rw ^ 1) * 4096 + lane * 16;
    if (isA) {
#pragma unroll
      for (int j = 0; j < 4; j++)
        st128(rs, v4u{pkbf(keep[4 * j], keep[4 * j + 1]), tag, pkbf(keep[4 * j + 2], keep[4 * j + 3]), tag}, WS_PART + lane * 16 + j * 1024, (b * 4 + rw) * 4096); TR(26);
      wait_prog(bprog, 13, cB);
      TR(27);
      v4u pr[4];
#pragma unroll 1
      for (unsigned i = 0; i < kMemSpin; ++i) {
        CFENCE();
#pragma unroll
        for (int j = 0; j < 4; j++) pr[j] = ld128(rs, WS_PART + lane * 16 + j * 1024, ((b ^ 1) * 4 + rw) * 4096);
        CFENCE();
        unsigned bad = 0;
#pragma unroll
        for (int j = 0; j < 4; j++) bad |= (pr[j].y ^ tag) | (pr[j].w ^ tag);
        if (wall(bad == 0)) break;
        __builtin_amdgcn_s_sleep(2);
      }
      CFENCE();
      TR(28);
      wait_prog(xprog, 9, cX);
      const float rstd = lds_ld<float>(lds0 + LD_RS + (32 * th + n32) * 4);
      const unsigned ay = lds0 + LD_UT + (32 * kh + 4 * hh + q) * 128 + ((th ^ (q >> 1)) << 6) + 32 * blk + 8 * pp;
#pragma unroll
      for (int j = 0; j < 4; j++) {
        const v2u y = __builtin_bit_cast(v2u, tr_ld(ay + j * 1024));
        lds_st<f4>(bx + j * 1024, f4{bflo(y.x) + rstd * bflo(pr[j].x), bfhi(y.x) + rstd * bfhi(pr[j].x),
                                     bflo(y.y) + rstd * bflo(pr[j].z), bfhi(y.y) + rstd * bfhi(pr[j].z)});
      }
      set_prog(myprog, 15);
      TR(29);
      ROLE_END();
    } else {
      const float rstd = lds_ld<float>(lds0 + LD_RS + (32 * th + n32) * 4);
#pragma unroll
      for (int j = 0; j < 4; j++) {
        keep[4 * j] = bflo(xres[j].x) + rstd * keep[4 * j]; keep[4 * j + 1] = bfhi(xres[j].x) + rstd * keep[4 * j + 1];
        keep[4 * j + 2] = bflo(xres[j].y) + rstd * keep[4 * j + 2]; keep[4 * j + 3] = bfhi(xres[j].y) + rstd * keep[4 * j + 3];
      } TR(26);
      wait_prog(aprog, 15, cA, 0x80u << (8 * rw));
      TR(27);
      v2u pk[4];
#pragma unroll
      for (int j = 0; j < 4; j++) {
        const f4 o = lds_ld<f4>(bx + j * 1024);
        pk[j].x = pkbf(keep[4 * j] + o[0], keep[4 * j + 1] + o[1]);
        pk[j].y = pkbf(keep[4 * j + 2] + o[2], keep[4 * j + 3] + o[3]);
      }
      u16* op = out + (size_t)(64 * tb + 32 * th + n32) * D_ + 64 * cb + 32 * kh + 8 * hh;
#pragma unroll
      for (int j = 0; j < 4; j += 2) {
        const auto r0 = __builtin_amdgcn_permlane32_swap(pk[j].x, pk[j + 1].x, false, false);
        const auto r1 = __builtin_amdgcn_permlane32_swap(pk[j].y, pk[j + 1].y, false, false);
        const unsigned e0 = r0[0], e1 = r1[0], e2 = r0[1], e3 = r1[1];
        *(v4u*)(op + 8 * j) = v4u{e0, e1, e2, e3};
      }
      TR(29);
      ROLE_END();
    }
  }
}

}
#if defined(__HIP_DEVICE_COMPILE__)
#pragma clang attribute pop
#endif

extern "C" void kernel_launch(void* const* d_in, const int* in_sizes, int n_in, void* d_out, int out_size, void* d_ws,
                              size_t ws_size, hipStream_t stream) {
  (void)in_sizes; (void)n_in; (void)out_size; (void)ws_size;
  (void)hipFuncSetAttribute((const void*)fused_kernel, hipFuncAttributeMaxDynamicSharedMemorySize, LDS_BYTES);
  fused_kernel<<<256, 1024, LDS_BYTES, stream>>>((const u16*)d_in[0], (const u16*)d_in[1], (const u16*)d_in[2], (const u16*)d_in[3],
                                                (const u16*)d_in[4], (const u16*)d_in[5], (const u16*)d_in[6], (const u16*)d_in[8],
                                                (const u16*)d_in[9], (u16*)d_out, (char*)d_ws);
}
```

```cpp
#define PADT 1800
#include <hip/hip_runtime.h>
#include <stdint.h>

#if defined(__HIP_DEVICE_COMPILE__)
#pragma clang attribute push(__attribute__((target("no-packed-fp32-ops"))), apply_to = function)
#endif
namespace {

typedef unsigned short u16;
typedef unsigned long long u64;
typedef short s4 __attribute__((ext_vector_type(4)));
typedef short s8 __attribute__((ext_vector_type(8)));
typedef short sh2 __attribute__((ext_vector_type(2)));
typedef __bf16 b2 __attribute__((ext_vector_type(2)));
typedef _Float16 h2 __attribute__((ext_vector_type(2)));
typedef _Float16 h8 __attribute__((ext_vector_type(8)));
typedef float f2 __attribute__((ext_vector_type(2)));
typedef float f4 __attribute__((ext_vector_type(4)));
typedef float f16v __attribute__((ext_vector_type(16)));
typedef unsigned v2u __attribute__((ext_vector_type(2)));
typedef unsigned v4u __attribute__((ext_vector_type(4)));
#define LAS __attribute__((address_space(3)))
#define DI __device__ __forceinline__

#define PRIO_L 2
#define PRIO_L2 1
#define PRIO_A 2
#define PRIO_B 1
#define PRIO_X 2
#define PRIO_X2 1
#define PSLEEP 6
#define XGATE 4
#define YGATE 11
constexpr int D_ = 512, F_ = 1792, N_ = 128;
#define CODE_LINES 200

constexpr int WS_EPOCH = 0;
constexpr int WS_YFLAG = 4096;
constexpr int WS_GRAN = 16384;
constexpr int WS_CSUM = 32768;
constexpr int WS_HFLAG = 65536;
constexpr int WS_XT = WS_HFLAG + 2048 * 128;
constexpr int WS_YT = WS_XT + 4 * 1024 * 1024;
constexpr int WS_PART = WS_YT + 2 * 1024 * 1024;
constexpr int WS_H = WS_PART + 4 * 1024 * 1024;
constexpr int WS_END = WS_H + 64 * 112 * 1024;
constexpr int WS_STAMP = 32 * 1024 * 1024;

constexpr int LD_KT = 131072;
constexpr int KT_COPY = 5120 + 64;
constexpr int KT_CH = 2 * KT_COPY;
constexpr int LD_UT = LD_KT + 2 * KT_CH;
constexpr int UT_CH = 4576;
constexpr int LD_CTL = LD_UT + 2 * UT_CH;
constexpr int LD_APROG = LD_CTL, LD_BPROG = LD_CTL + 4, LD_LPROG = LD_CTL + 8, LD_XPROG = LD_CTL + 12, LD_TAG = LD_CTL + 16;
constexpr int LD_RS = LD_CTL + 64;
constexpr int LD_DUMP = LD_RS + 256;
constexpr int LD_END = LD_DUMP + 1024;
constexpr int LDS_BYTES = 163840;
static_assert(LD_END <= LDS_BYTES, "lds");
static_assert(LD_UT % 16 == 0 && LD_CTL % 16 == 0 && KT_COPY % 16 == 0, "align");

constexpr unsigned kLdsSpin = 1u << 18, kMemSpin = 1u << 13;

DI float u2f(unsigned v) { return __builtin_bit_cast(float, v); }
DI unsigned f2u(float v) { return __builtin_bit_cast(unsigned, v); }
DI float bflo(unsigned v) { return u2f(v << 16); }
DI float bfhi(unsigned v) { return u2f(v & 0xffff0000u); }
DI unsigned pkbf(float a, float b) { f2 v = {a, b}; return __builtin_bit_cast(unsigned, __builtin_convertvector(v, b2)); }
DI unsigned pkh(float a, float b) { f2 v = {a, b}; return __builtin_bit_cast(unsigned, __builtin_convertvector(v, h2)); }
DI float rbf(float a) { return bflo(pkbf(a, a)); }
DI float dot2(unsigned a, unsigned b, float acc) {
  return __builtin_amdgcn_fdot2_f32_bf16(__builtin_bit_cast(b2, a), __builtin_bit_cast(b2, b), acc, false);
}
DI unsigned umin(unsigned a, unsigned b) { return a < b ? a : b; }
DI unsigned umax(unsigned a, unsigned b) { return a > b ? a : b; }
DI bool wall(bool p) { return __builtin_amdgcn_ballot_w64(!p) == 0; }
template <class T> DI T lds_ld(unsigned a) { return *(LAS T*)(a); }
template <class T> DI void lds_st(unsigned a, T v) { *(LAS T*)(a) = v; }
DI s4 tr_ld(unsigned a) { return __builtin_amdgcn_ds_read_tr16_b64_v4i16((LAS s4*)(a)); }
DI s8 cat(s4 a, s4 b) { return __builtin_shufflevector(a, b, 0, 1, 2, 3, 4, 5, 6, 7); }
DI f16v mfma32(s8 a, s8 b, f16v c) { return __builtin_amdgcn_mfma_f32_32x32x16_bf16(a, b, c, 0, 0, 0); }
DI f4 mfma16(s8 a, s8 b, f4 c) { return __builtin_amdgcn_mfma_f32_16x16x32_bf16(a, b, c, 0, 0, 0); }
DI f4 mfma16h(v4u a, v4u b, f4 c) {
  return __builtin_amdgcn_mfma_f32_16x16x32_f16(__builtin_bit_cast(h8, a), __builtin_bit_cast(h8, b), c, 0, 0, 0);
}
#define SCHED() __builtin_amdgcn_sched_barrier(0)
#define CFENCE() asm volatile("" ::: "memory")
#define WAITV(n) asm volatile("s_waitcnt vmcnt(" #n ")" ::: "memory")

#define SPIN_SLEEP
DI void set_prog(unsigned addr, unsigned v) { asm volatile("ds_write_b8 %0, %1" ::"v"(addr), "v"(v | 0x80u) : "memory"); }
DI void wait_prog(unsigned addr, unsigned need, unsigned& cache, unsigned mask = 0x80808080u) {
  unsigned t, cnt;
  cache = __builtin_amdgcn_readfirstlane(cache);
  asm volatile(
      "s_mov_b32 %[cnt], %[lim]\n"
      "1:\n\t"
      "s_sub_u32 %[t], %[c], %[n]\n\t"
      "s_andn2_b32 %[t], %[m], %[t]\n\t"
      "s_cbranch_scc0 2f\n\t"
      SPIN_SLEEP
      "ds_read_b32 v127, %[a]\n\t"
      "s_waitcnt lgkmcnt(0)\n\t"
      "v_readfirstlane_b32 %[c], v127\n\t"
      "s_sub_u32 %[cnt], %[cnt], 1\n\t"
      "s_cbranch_scc0 1b\n"
      "2:"
      : [c] "+&s"(cache), [t] "=&s"(t), [cnt] "=&s"(cnt)
      : [a] "v"(addr), [n] "s"(need * 0x01010101u), [m] "s"(mask), [lim] "i"(kLdsSpin)
      : "memory", "scc", "v127");
}
DI bool prog_ok(unsigned cache, unsigned need) { return (0x80808080u & ~(cache - need * 0x01010101u)) == 0; }
DI void glds(unsigned voff, const char* sbase, unsigned m0v) {
  asm volatile("s_mov_b32 m0, %2\n\ts_nop 0\n\tglobal_load_lds_dwordx4 %0, %1" ::"v"(voff), "s"(sbase), "s"(m0v) : "memory");
}
constexpr int SC1 = 16;
constexpr int OOB = 0x7ffffff0;
typedef __amdgpu_buffer_rsrc_t rsrc_t;
DI v4u ld128(rsrc_t r, int voff, int soff) { return __builtin_amdgcn_raw_buffer_load_b128(r, voff, soff, SC1); }
DI v2u ld64(rsrc_t r, int voff, int soff) { return __builtin_amdgcn_raw_buffer_load_b64(r, voff, soff, SC1); }
DI unsigned ld32(rsrc_t r, int voff, int soff) { return __builtin_amdgcn_raw_buffer_load_b32(r, voff, soff, SC1); }
DI void st128(rsrc_t r, v4u v, int voff, int soff) {
  __builtin_amdgcn_raw_buffer_store_b128(v, r, voff, soff, SC1);
  asm volatile("s_nop 1" ::"v"(v) : "memory");
}
DI void st64(rsrc_t r, v2u v, int voff, int soff) { __builtin_amdgcn_raw_buffer_store_b64(v, r, voff, soff, SC1); }
DI void st32(rsrc_t r, unsigned v, int voff, int soff) { __builtin_amdgcn_raw_buffer_store_b32(v, r, voff, soff, SC1); }
DI v4u ldp128(rsrc_t r, int voff, int soff) { return __builtin_amdgcn_raw_buffer_load_b128(r, voff, soff, 0); }
DI void poll_tag(rsrc_t r, int voff, unsigned tag) {
#pragma unroll 1
  for (unsigned i = 0; i < kMemSpin; ++i) {
    CFENCE();
    const unsigned v = ld32(r, voff, 0);
    if (wall(v == tag)) break;
    __builtin_amdgcn_s_sleep(PSLEEP);
  }
  CFENCE();
}
template <int CTRL> DI float dpp(float v) { return u2f(__builtin_amdgcn_update_dpp(0u, f2u(v), CTRL, 0xf, 0xf, true)); }
DI float sum16(float v) { v += dpp<0xB1>(v); v += dpp<0x4E>(v); v += dpp<0x141>(v); return v + dpp<0x140>(v); }
DI float add_x16(float v) { const auto r = __builtin_amdgcn_permlane16_swap(f2u(v), f2u(v), false, false); const unsigned a = r[0], b = r[1]; return u2f(a) + u2f(b); }
DI float add_x32(float v) { const auto r = __builtin_amdgcn_permlane32_swap(f2u(v), f2u(v), false, false); const unsigned a = r[0], b = r[1]; return u2f(a) + u2f(b); }
template <int CTRL> DI unsigned dppmax(unsigned v) { return umax(v, __builtin_amdgcn_update_dpp(0u, v, CTRL, 0xf, 0xf, true)); }
DI unsigned wave_umax(unsigned v) {
  v = dppmax<0x140>(dppmax<0x141>(dppmax<0x4E>(dppmax<0xB1>(v))));
  return umax(umax(__builtin_amdgcn_readlane(v, 0), __builtin_amdgcn_readlane(v, 16)), umax(__builtin_amdgcn_readlane(v, 32), __builtin_amdgcn_readlane(v, 48)));
}
struct cpx { float r, i; };
DI cpx cmul(cpx a, cpx b) { return {a.r * b.r - a.i * b.i, a.r * b.i + a.i * b.r}; }
DI cpx cexp_at(float lr, float rev, float p) {
  const float e = __builtin_amdgcn_exp2f(lr * p);
  const float f = __builtin_amdgcn_fractf(rev * p);
  return {e * __builtin_amdgcn_cosf(f), e * __builtin_amdgcn_sinf(f)};
}
constexpr float kLog2e = 1.44269504088896341f;

#define TR(i)
struct Geo {
  unsigned lds0; int lane, rw, hh, blk, q, pp, g, n16, n32;
};

template <bool IS_A, class F>
DI void gemm1_loop(const Geo& G, f16v (&acc)[2], unsigned& cL, F&& hook) {
  const int th = G.rw >> 1, kh = G.rw & 1;
  const unsigned lprog = G.lds0 + LD_LPROG, myprog = G.lds0 + (IS_A ? LD_APROG : LD_BPROG) + G.rw;
  unsigned ax[4], aw[2];
#pragma unroll
  for (int s = 0; s < 4; s++) ax[s] = G.lds0 + ((IS_A ? 0 : 64) + 32 * th + G.n32) * 128 + (((2 * s + G.hh) ^ ((G.n32 >> 1) & 7)) << 4);
#pragma unroll
  for (int ft = 0; ft < 2; ft++) aw[ft] = G.lds0 + 16384 + (8 * G.hh + G.q) * 256 + (((2 * kh + ft) ^ G.q) << 6) + 32 * G.blk + 8 * G.pp;
  constexpr int PD = 2;
  s8 fx[PD + 1], fw[PD + 1][2];
  auto rdx = [&](int i) { fx[i % (PD + 1)] = lds_ld<s8>(ax[i & 3] + ((i >> 2) & 1) * 32768); };
  auto rdw = [&](int i, int ft) {
    const unsigned a = aw[ft] + ((i >> 2) & 1) * 32768 + (i & 3) * 4096;
    fw[i % (PD + 1)][ft] = cat(tr_ld(a), tr_ld(a + 1024));
  };
  wait_prog(lprog, 1, cL);
  hook(16);
#pragma unroll
  for (int i = 0; i < PD; i++) { rdx(i); rdw(i, 0); rdw(i, 1); }
  unsigned pv = 0;
#pragma unroll
  for (int i = 0; i < 32; i++) {
    const bool p = i + PD < 32;
    SCHED();
    if ((i & 3) == 2) {
      const int s = i >> 2;
      if (p) cL = __builtin_amdgcn_readfirstlane(pv);
      SCHED();
      set_prog(myprog, s + 2);
      hook(s);
      if (p) wait_prog(lprog, (IS_A || s < 3) ? s + 2 : (s < 5 ? 9 : 10), cL);
      hook(s + 8);
      if (p && (s & 1)) {
#pragma unroll
        for (int t = 0; t < 4; t++) ax[t] ^= 65536;
        aw[0] ^= 65536; aw[1] ^= 65536;
      }
      SCHED();
    }
    acc[0] = mfma32(fw[i % (PD + 1)][0], fx[i % (PD + 1)], acc[0]);
    SCHED();
    if ((i & 3) == 0 && i < 28) pv = *(volatile LAS unsigned*)lprog;
    if (p) { rdx(i + PD); rdw(i + PD, 0); }
    SCHED();
    acc[1] = mfma32(fw[i % (PD + 1)][1], fx[i % (PD + 1)], acc[1]);
    SCHED();
    if (p) rdw(i + PD, 1);
  }
  SCHED();
}

__global__ __launch_bounds__(1024, 4) void fused_kernel(
    const u16* __restrict__ x, const u16* __restrict__ logA, const u16* __restrict__ Aim, const u16* __restrict__ Cre,
    const u16* __restrict__ Cim, const u16* __restrict__ logdt, const u16* __restrict__ Dsk, const u16* __restrict__ w1,
    const u16* __restrict__ w2, u16* __restrict__ out, char* ws) {
  extern __shared__ __attribute__((aligned(16))) char smem[];
  u64 pc0;
  asm volatile("s_getpc_b64 %0" : "=s"(pc0));
  const unsigned lds0 = (unsigned)(size_t)(LAS char*)smem;
  const int tid = __builtin_amdgcn_workitem_id_x(), lane = tid & 63;
  const int w = __builtin_amdgcn_readfirstlane(tid >> 6), role = w >> 2, rw = w & 3;
  const int b = __builtin_amdgcn_workgroup_id_x(), xcd = b & 7, idx = b >> 3;
  const int fh = xcd & 1, pl = idx & 3, cb = idx >> 2;
  const bool has1 = cb < 7;
  const int panel = 4 * (xcd >> 1) + pl, slice = 7 * fh + cb;
  const int tb = 2 * panel + fh;
  const rsrc_t rs = __builtin_amdgcn_make_buffer_rsrc(ws, 0, WS_END, 0x00020000);
  const int hh = lane >> 5, blk = (lane >> 4) & 1, q = (lane & 15) >> 2, pp = lane & 3, g = lane >> 4, n16 = lane & 15, n32 = lane & 31;
  const unsigned lprog = lds0 + LD_LPROG, aprog = lds0 + LD_APROG, bprog = lds0 + LD_BPROG, xprog = lds0 + LD_XPROG;
  unsigned cL = 0x80808080u, cA = 0x80808080u, cB = 0x80808080u, cX = 0x80808080u;
  u64 padt0;
  asm volatile("s_memrealtime %0" : "=s"(padt0)::"memory");
  unsigned pA, pI, pCr, pCi, pf;
  v4u i0, i1, i2, i3;
  lds_st<unsigned>(lds0 + LD_CTL + (tid & 3) * 4, 0x80808080u);
  {
    const unsigned po = (unsigned)((2 * b * N_ + 64 * rw + lane) * 2);
    const unsigned co = umin(idx * 8 + (lane & 7), CODE_LINES - 1) * 128;
    const unsigned ro = (unsigned)((64 * tb + 8 * cb + 2 * rw + hh) * 1024 + n32 * 16);
    const unsigned to = (unsigned)((64 * tb + 32 * (rw >> 1) + (lane >> 2)) * 1024 + 128 * cb + 64 * (rw & 1) + (lane & 3) * 16);
    asm volatile(
        "s_cmp_ge_u32 %[role], 2\n\t"
        "s_cbranch_scc1 2f\n\t"
        "s_cmp_eq_u32 %[role], 1\n\t"
        "s_cbranch_scc1 1f\n\t"
        "global_load_ushort %[a0], %[po], %[pA]\n\t"
        "global_load_ushort %[a1], %[po], %[pI]\n\t"
        "global_load_ushort %[a2], %[po], %[pCr]\n\t"
        "global_load_ushort %[a3], %[po], %[pCi]\n\t"
        "global_load_dword %[a4], %[co], %[pc]\n\t"
        "s_branch 2f\n"
        "1:\n\t"
        "global_load_dwordx4 %[i2], %[to], %[x]\n\t"
        "global_load_dwordx4 %[i3], %[t1], %[x]\n\t"
        "global_load_dwordx4 %[i0], %[ro], %[x]\n\t"
        "global_load_dwordx4 %[i1], %[ro], %[x] offset:512\n"
        "2:"
        : [i0] "=&v"(i0), [i1] "=&v"(i1), [i2] "=&v"(i2), [i3] "=&v"(i3), [a0] "=&v"(pA), [a1] "=&v"(pI), [a2] "=&v"(pCr), [a3] "=&v"(pCi),
          [a4] "=&v"(pf)
        : [role] "s"(role), [po] "v"(po), [co] "v"(co), [ro] "v"(ro), [to] "v"(to), [t1] "v"(to + 16384), [x] "s"(x), [pA] "s"(logA), [pI] "s"(Aim),
          [pCr] "s"(Cre), [pCi] "s"(Cim), [pc] "s"(pc0 & ~127ull), "s"(w1), "s"(w2), "s"(out), "s"(ws), "s"(logdt), "s"(Dsk)
        : "memory", "scc");
  }
#define ROLE_END() do { if (role == 2) { for (int i = 0; i < 100000; i++) { u64 t; asm volatile("s_sleep 8\n\ts_memrealtime %0\n\ts_waitcnt lgkmcnt(0)" : "=s"(t)::"memory"); if (t - padt0 >= PADT) break; } } __builtin_amdgcn_endpgm(); __builtin_unreachable(); } while (0)
#define START_BARRIER() asm volatile("s_waitcnt lgkmcnt(0)\n\ts_barrier" ::: "memory")

  if (__builtin_expect(role == 2, 1)) {
    __builtin_amdgcn_s_setprio(PRIO_L);
    const unsigned myprog = lprog + rw;
    if (__builtin_expect(has1, 1)) {
      const int rot1 = (cb + 2 * pl) & 7;
      unsigned xo[2];
#pragma unroll
      for (int j = 0; j < 2; j++) xo[j] = (unsigned)((8 * j + (lane >> 3)) * 1024 + (((lane & 7) ^ ((4 * j + g) & 7)) << 4));
      const unsigned wo = (unsigned)(g * 3584 + ((n16 ^ (g << 2)) << 4));
      const char* xa = (const char*)x + (size_t)((tb ^ 1) * 64 + 16 * rw) * 1024;
      const char* xb = (const char*)x + (size_t)(tb * 64 + 16 * rw) * 1024;
      const char* wb = (const char*)w1 + (size_t)(16 * rw) * 3584 + slice * 256;
      auto issue_x = [&](int s, int grp) {
        const int ss = (s + rot1) & 7;
#pragma unroll
        for (int j = 0; j < 2; j++) glds(xo[j], (grp ? xb : xa) + ss * 128, lds0 + (s & 3) * 32768 + grp * 8192 + rw * 2048 + j * 1024);
      };
      auto issue_w = [&](int s) {
        const int ss = (s + rot1) & 7;
#pragma unroll
        for (int j = 0; j < 4; j++) glds(wo, wb + (size_t)(ss * 64 + 4 * j) * 3584, lds0 + (s & 3) * 32768 + 16384 + rw * 4096 + j * 1024);
      };
      TR(1);
      issue_x(0, 0); issue_w(0); issue_x(0, 1);
      START_BARRIER();
#pragma unroll 1
      for (int s = 1; s < 8; s++) {
        if (s >= 3 && !(prog_ok(cB, s - 2) && (s == 3 || prog_ok(cA, s - 2)))) {
          WAITV(0);
          set_prog(myprog, s);
          wait_prog(bprog, s - 2, cB);
          if (s > 3) wait_prog(aprog, s - 2, cA);
        }
        TR(2 * s);
        issue_x(s, 0); issue_w(s);
        if (s < 4) issue_x(s, 1);
        const v2u pv = *(volatile LAS v2u*)aprog;
        if (s < 4) WAITV(8); else WAITV(6);
        set_prog(myprog, s);
        TR(2 * s + 1);
        cA = __builtin_amdgcn_readfirstlane(pv.x); cB = __builtin_amdgcn_readfirstlane(pv.y);
      }
#pragma unroll
      for (int s = 4; s < 8; s++) issue_x(s, 1);
    } else {
      const int u = (xcd >> 1) * 4 + pl, unit = 14 * fh + umin(u, 13);
      const rsrc_t rw1 = __builtin_amdgcn_make_buffer_rsrc((void*)w1, 0, F_ * D_ * 2, 0x00020000);
      const rsrc_t rw2 = __builtin_amdgcn_make_buffer_rsrc((void*)w2, 0, F_ * D_ * 2, 0x00020000);
      v4u t[16];
      START_BARRIER();
#pragma unroll
      for (int j = 0; j < 16; j++) t[j] = ldp128(rw1, (128 * rw + (lane >> 3)) * 3584 + unit * 128 + (lane & 7) * 16, j * 8 * 3584);
      float a[8];
      unsigned sel_lo = 0x00003f80u, sel_hi = 0x3f800000u;
      asm volatile("" : "+v"(sel_lo), "+v"(sel_hi));
#pragma unroll
      for (int e = 0; e < 8; e++) a[e] = 0.f;
#pragma unroll
      for (int j = 0; j < 16; j++)
#pragma unroll
        for (int e = 0; e < 4; e++) { a[2 * e] = dot2(t[j][e], sel_lo, a[2 * e]); a[2 * e + 1] = dot2(t[j][e], sel_hi, a[2 * e + 1]); }
      lds_st<f4>(lds0 + rw * 2048 + lane * 32, f4{a[0], a[1], a[2], a[3]});
      lds_st<f4>(lds0 + rw * 2048 + lane * 32 + 16, f4{a[4], a[5], a[6], a[7]});
      set_prog(myprog, 1);
      wait_prog(lprog, 1, cL);
      float sm = 0.f;
#pragma unroll
      for (int j = 0; j < 8; j++) sm += lds_ld<float>(lds0 + (8 * g + j) * 256 + (16 * rw + n16) * 4);
      sm = add_x32(add_x16(sm));
      set_prog(myprog, 2);
      wait_prog(bprog, 1, cB);
      const unsigned tag = lds_ld<unsigned>(lds0 + LD_TAG);
      st64(rs, v2u{f2u(sm), tag}, (lane < 16 && u < 14) ? lane * 8 : OOB, WS_CSUM + (unit * 64 + 16 * rw) * 8);
      TR(2);
      wait_prog(xprog, 2, cX);
      unsigned tt[7];
#pragma unroll
      for (int j = 0; j < 7; j++) tt[j] = __builtin_amdgcn_raw_buffer_load_b32(rw2, lane * 128, (896 * fh + 224 * pl + 32 * j + 8 * rw) * 1024, 0);
#pragma unroll
      for (int j = 0; j < 7; j++) asm volatile("" ::"v"(tt[j]));
      wait_prog(lprog, 2, cL);
    }
    {
      const rsrc_t rw2 = __builtin_amdgcn_make_buffer_rsrc((void*)w2, 0, F_ * D_ * 2, 0x00020000);
      const int wo2 = (896 * fh + 32 * rw + (lane >> 3)) * 1024 + cb * 128 + (lane & 7) * 16;
      v4u wq[7][4];
      auto ldw2 = [&](int u) {
        int sg = u + 2 * pl; sg = sg >= 7 ? sg - 7 : sg;
#pragma unroll
        for (int j = 0; j < 4; j++) wq[u][j] = ldp128(rw2, wo2, sg * 131072 + j * 8192);
        CFENCE();
      };
      if (has1) { WAITV(8); set_prog(myprog, 8); }
      TR(16);
      ldw2(0);
      if (has1) { WAITV(8); set_prog(myprog, 9); }
      TR(17);
      ldw2(1);
      if (has1) { WAITV(8); set_prog(myprog, 10); } else set_prog(myprog, 10);
      TR(18);
      __builtin_amdgcn_s_setprio(PRIO_L2);
#pragma unroll
      for (int u = 2; u < 7; u++) ldw2(u);
      set_prog(myprog, 11);
      TR(19);
      const unsigned wdst = lds0 + rw * 4096 + (lane >> 3) * 128 + (((lane & 7) ^ (((lane >> 4) & 1) << 2)) << 4);
#pragma unroll
      for (int u = 0; u < 7; u++) {
        if (!(u & 1)) { wait_prog(bprog, 6 + (u >> 1), cB); wait_prog(aprog, 6 + (u >> 1), cA); TR(20 + (u >> 1)); }
#pragma unroll
        for (int j = 0; j < 4; j++) lds_st<v4u>(wdst + u * 16384 + j * 1024, wq[u][j]);
      }
      set_prog(myprog, 15);
    }
    ROLE_END();
  } else if (role == 0) {
    __builtin_amdgcn_s_setprio(PRIO_X);
    const unsigned myprog = xprog + rw, pmask = 0x80u << (8 * (rw ^ 1));
    const int cp = b, c = rw >> 1, mh = rw & 1, ch = 2 * cp + c;
    START_BARRIER();
    unsigned sdt, sD;
    asm volatile("s_load_dword %0, %2, %4\n\ts_load_dword %1, %3, %4\n\ts_waitcnt lgkmcnt(0)"
                 : "=&s"(sdt), "=&s"(sD) : "s"(logdt), "s"(Dsk), "s"(b * 4) : "memory");
    const unsigned pdt = c ? sdt >> 16 : sdt & 0xffffu, pD = c ? sD >> 16 : sD & 0xffffu;
    asm volatile("s_waitcnt vmcnt(1)" : "+v"(pA), "+v"(pI), "+v"(pCr), "+v"(pCi), "+v"(pf)::"memory");
    const unsigned scr = lds0 + LD_KT + c * KT_CH + mh * KT_COPY;
    const unsigned oscr = lds0 + LD_KT + c * KT_CH + (mh ^ 1) * KT_COPY;
    f4 kd[3];
    {
      const float dt = rbf(__builtin_amdgcn_exp2f(bflo(pdt) * kLog2e));
      const float lr = -rbf(__builtin_amdgcn_exp2f(bflo(pA) * kLog2e)) * dt * kLog2e;
      const float rev = bflo(pI) * dt * 0.15915494309189535f;
      TR(1);
      cpx C = {bflo(pCr) * dt, bflo(pCi) * dt};
      const unsigned eb = umin(umax(wave_umax(umax(f2u(C.r) & 0x7fffffffu, f2u(C.i) & 0x7fffffffu)) >> 23, 12u), 254u);
      const float sdn = u2f((266u - eb) << 23), sup = u2f((eb - 11u) << 23);
      C.r *= sdn; C.i *= -sdn;
      const cpx e8 = cexp_at(lr, rev, 8.f);
      const cpx e1 = cexp_at(lr, -rev, 1.f), e2 = cmul(e1, e1), e4 = cmul(e2, e2), gg = cexp_at(lr, -rev, 128.f);
      const unsigned wa = scr + lane * 4, ra = scr + n16 * 272 + g * 16;
      auto put = [&](int i, cpx v) { lds_st<unsigned>(wa + i * 272, pkh(v.r, v.i)); };
      v4u fq[4];
      {
        const cpx e16 = cmul(e8, e8);
        cpx a = e16, bq = cmul(e8, e16);
        put(0, cpx{1.f, 0.f}); put(1, e8);
#pragma unroll
        for (int j = 2; j < 16; j += 2) {
          put(j, a); put(j + 1, bq);
          if (j < 14) { a = cmul(a, e16); bq = cmul(bq, e16); }
        }
        CFENCE();
#pragma unroll
        for (int s = 0; s < 4; s++) fq[s] = lds_ld<v4u>(ra + s * 64);
        CFENCE();
      }
      auto tile = [&](int T) {
        CFENCE();
        v4u fp[4];
#pragma unroll
        for (int s = 0; s < 4; s++) fp[s] = lds_ld<v4u>(ra + s * 64);
        CFENCE();
        f4 a = {0.f, 0.f, 0.f, 0.f};
#pragma unroll
        for (int s = 0; s < 4; s++) a = mfma16h(fp[s], fq[s], a);
        kd[T] = a * sup;
      };
      cpx P = C;
      {
        cpx a = P, bq = cmul(P, gg);
        P = cmul(bq, gg);
#pragma unroll
        for (int j = 0; j < 8; j++) {
          put(j, a); put(8 + j, bq);
          if (j < 7) { a = cmul(a, e1); bq = cmul(bq, e1); }
        }
      }
      tile(0);
      const cpx P2 = P, P3 = cmul(P, gg), P4 = cmul(P3, gg);
      P = P4;
#pragma unroll
      for (int j = 0; j < 12; j++) {
        put(4 * (j & 3) + (j < 4 ? 0 : 1 + (j >> 2)), P);
        if (j < 4) put(4 * j + 1, cmul(P, e4));
        P = cmul(P, gg);
      }
      tile(1);
      {
        cpx a = P2, bq = P3;
#pragma unroll
        for (int j = 0; j < 4; j++) {
          put(j, a); put(4 + j, bq);
          if (j < 3) { a = cmul(a, e2); bq = cmul(bq, e2); }
        }
        put(8, P4); put(9, P);
      }
      tile(2);
    }
    asm volatile("s_waitcnt vmcnt(0)" : "+v"(pf)::"memory");
    TR(2);
    wait_prog(lprog, has1 ? XGATE : 2, cL);
    const int tok0 = 512 * rw + 8 * lane;
    v4u gq[4], xv[4];
    auto gather = [&]() {
#pragma unroll
      for (int j = 0; j < 4; j++) gq[j] = ld128(rs, WS_GRAN + lane * 16, (rw * 4 + j) * 1024);
#pragma unroll
      for (int j = 0; j < 4; j++) xv[j] = ld128(rs, (tok0 >> 5) * 128 + (lane & 3) * 16 + (j >> 1) * 64, WS_XT + cp * 16384 + (j & 1) * 8192);
      CFENCE();
    };
    TR(10);
    gather();
    TR(11);
    const unsigned ut = lds0 + LD_UT + c * UT_CH, kt = lds0 + LD_KT + c * KT_CH;
    if (mh == 0) {
      lds_st<f4>(scr + 1024 + lane * 16, kd[1]);
      lds_st<float>(scr + 2048 + lane * 4, kd[2][1]);
      lds_st<v4u>(ut + umin(lane, 29) * 16, v4u{0u, 0u, 0u, 0u});
      set_prog(myprog, 2);
      wait_prog(xprog, 2, cX, pmask);
      f4 v = kd[0] + lds_ld<f4>(oscr + 3072 + lane * 16);
      const f4 u = kd[2] + lds_ld<f4>(oscr + 4096 + lane * 16);
      CFENCE();
      v[0] += lane == 0 ? bflo(pD) : 0.f;
      const float nx = u2f(__builtin_amdgcn_update_dpp(0u, f2u(u[0]), 0x130, 0xf, 0xf, false));
      const unsigned a1 = lane < 32 ? kt + 3568 - 16 * lane : lds0 + LD_DUMP;
      const unsigned u22 = pkbf(u[2], u[2]), u00 = pkbf(u[0], u[0]);
      lds_st<v4u>(a1, v4u{pkbf(nx, u[3]), u22, pkbf(u[2], u[1]), u00});
      lds_st<v4u>(lane < 32 ? a1 + KT_COPY : a1, v4u{pkbf(u[3], u[2]), u22, pkbf(u[1], u[0]), u00});
      CFENCE();
      const unsigned a0 = kt + 4088 - 2 * (128 * (g >> 1) + 8 * n16 + 4 * (g & 1));
      const unsigned v32 = pkbf(v[3], v[2]), v10 = pkbf(v[1], v[0]);
      lds_st<v2u>(a0, v2u{v32, v10});
      lds_st<u16>(a0 + KT_COPY - 2, (u16)v32);
      lds_st<unsigned>(a0 + KT_COPY, pkbf(v[2], v[1]));
      lds_st<u16>(a0 + KT_COPY + 4, (u16)(v10 >> 16));
      lds_st<u16>(kt + KT_COPY + 4094, (u16)0);
      lds_st<unsigned>(kt + 4096 + (lane & 7) * 4 + ((lane >> 3) & 1) * KT_COPY, 0u);
    } else {
      lds_st<f4>(scr + 3072 + lane * 16, kd[0]);
      lds_st<f4>(scr + 4096 + lane * 16, kd[2]);
      set_prog(myprog, 2);
      wait_prog(xprog, 2, cX, pmask);
      const f4 v = kd[1] + lds_ld<f4>(oscr + 1024 + lane * 16);
      const float kend = u2f(__builtin_amdgcn_readlane(f2u(kd[2][1]), 32)) + lds_ld<float>(oscr + 2048 + 128);
      CFENCE();
      auto next = [&](float cur, unsigned last) { return u2f(__builtin_amdgcn_update_dpp(last, f2u(cur), 0x130, 0xf, 0xf, false)); };
      auto rol = [&](float t) { return __builtin_amdgcn_update_dpp(0u, f2u(t), 0x134, 0xf, 0xf, false); };
      const float n0 = next(v[0], rol(v[2])), n2 = next(v[2], rol(v[3])), n3 = next(v[3], f2u(kend));
      const unsigned a1 = kt + 1008 - 16 * lane;
      {
        const unsigned v11 = pkbf(v[1], v[1]), v00 = pkbf(v[0], v[0]);
        lds_st<v4u>(a1 + 2048, v4u{pkbf(n0, n0), v11, pkbf(v[1], v[0]), v00});
        lds_st<v4u>(a1 + 2048 + KT_COPY, v4u{pkbf(n0, v[1]), v11, v00, v00});
      }
#pragma unroll
      for (int r = 2; r < 4; r++) {
        const float nx = r == 2 ? n2 : n3, z = (lane & 1) ? nx : v[r];
        const unsigned nn = pkbf(nx, nx), vv = pkbf(v[r], v[r]);
        lds_st<v4u>(a1 + (3 - r) * 1024, v4u{nn, pkbf(nx, z), vv, vv});
        lds_st<v4u>(a1 + (3 - r) * 1024 + KT_COPY, v4u{nn, pkbf(z, v[r]), vv, vv});
      }
    }
    TR(3);
    wait_prog(bprog, 1, cB);
    const unsigned tag = lds_ld<unsigned>(lds0 + LD_TAG);
    auto gathered = [&]() {
      unsigned bad = 0;
#pragma unroll
      for (int j = 0; j < 4; j++) bad |= (gq[j].y ^ tag) | (gq[j].w ^ tag) | (xv[j].y ^ tag) | (xv[j].w ^ tag);
      return wall(bad == 0);
    };
    if (!gathered()) {
#pragma unroll 1
      for (unsigned i = 0; i < kMemSpin; ++i) {
        __builtin_amdgcn_s_sleep(8);
        TR(12);
        gather();
        if (gathered()) break;
      }
    }
    TR(4);
    {
      float mr[8], ri[8];
#pragma unroll
      for (int j = 0; j < 4; j++) {
        ri[2 * j] = __builtin_amdgcn_rcpf(bfhi(gq[j].x)); mr[2 * j] = -bflo(gq[j].x) * ri[2 * j];
        ri[2 * j + 1] = __builtin_amdgcn_rcpf(bfhi(gq[j].z)); mr[2 * j + 1] = -bflo(gq[j].z) * ri[2 * j + 1];
      }
#pragma unroll
      for (int k = 0; k < 2; k++) {
        v4u o;
#pragma unroll
        for (int j = 0; j < 4; j++) {
          const unsigned d = xv[2 * k + (j >> 1)][2 * (j & 1)];
          o[j] = pkbf(bflo(d) * ri[2 * j] + mr[2 * j], bfhi(d) * ri[2 * j + 1] + mr[2 * j + 1]);
        }
        lds_st<v4u>(lds0 + LD_UT + k * UT_CH + 480 + tok0 * 2, o);
      }
      set_prog(myprog, 5);
      if ((tb >> 3) == rw) {
        const unsigned ar = lds0 + ((tok0 >> 6) == tb ? LD_RS : LD_DUMP) + (tok0 & 63) * 4;
        lds_st<f4>(ar, f4{ri[0], ri[1], ri[2], ri[3]});
        lds_st<f4>(ar + 16, f4{ri[4], ri[5], ri[6], ri[7]});
      }
      wait_prog(xprog, 5, cX);
      if (b == 0 && rw == 0) st32(rs, tag, lane == 0 ? 0 : OOB, WS_EPOCH);
    }
    TR(5);
    __builtin_amdgcn_s_setprio(PRIO_X2);
    f4 ya[8];
#pragma unroll
    for (int i = 0; i < 8; i++) ya[i] = f4{0.f, 0.f, 0.f, 0.f};
    {
      const unsigned ka = lds0 + LD_KT + c * KT_CH + (1 - (n16 & 1)) * KT_COPY + 4 * (1015 - (n16 >> 1) + 4 * g) - 64 * (59 + 4 * mh);
      const unsigned ua = ut + 2 * (224 + 16 * n16 + 8 * g) - 64 * (3 + 4 * mh);
      unsigned kb[4];
#pragma unroll
      for (int j = 0; j < 4; j++) { kb[j] = ka + 1024 * j; asm volatile("" : "+v"(kb[j])); }
      typedef unsigned v2a __attribute__((ext_vector_type(2), aligned(4)));
      v2u kl[2][8], kh_[2][8];
      s8 uf[2][8];
#define KL(r, k) kl[(r) & 1][k] = *(LAS v2a*)(kb[(7 - (k)) >> 1] + 64 * (3 - (r)) + 512 * ((7 - (k)) & 1))
#define KH(r, k) kh_[(r) & 1][k] = *(LAS v2a*)(kb[(7 - (k)) >> 1] + 64 * (3 - (r)) + 512 * ((7 - (k)) & 1) + 8)
#define UL(r, e) uf[(r) & 1][e] = lds_ld<s8>(ua + 64 * (3 - (r)) + 512 * (e))
#define MM(r, k, e) SCHED(); ya[(k) + (e)] = mfma16(__builtin_bit_cast(s8, v4u{kl[(r) & 1][k].x, kl[(r) & 1][k].y, kh_[(r) & 1][k].x, kh_[(r) & 1][k].y}), uf[(r) & 1][e], ya[(k) + (e)]); SCHED()
      KL(0, 0); KH(0, 0); UL(0, 7); UL(0, 6); UL(0, 5); UL(0, 4); UL(0, 3); UL(0, 2); UL(0, 1); UL(0, 0); KL(0, 1); KH(0, 1);
      MM(0, 0, 7); MM(0, 0, 6); MM(0, 0, 5); MM(0, 0, 4); MM(0, 0, 3); KH(0, 2); MM(0, 0, 2); KL(0, 2); MM(0, 0, 1); MM(0, 0, 0);
      MM(0, 1, 6); MM(0, 1, 5); MM(0, 1, 4); KH(0, 3); MM(0, 1, 3); KL(0, 3); MM(0, 1, 2); MM(0, 1, 1); MM(0, 1, 0);
      MM(0, 2, 5); KH(0, 4); MM(0, 2, 4); KL(0, 4); MM(0, 2, 3); KH(0, 5); MM(0, 2, 2); KL(0, 5); MM(0, 2, 1); KH(0, 6);
      MM(0, 2, 0); KL(0, 6); MM(0, 3, 4); KH(0, 7); MM(0, 3, 3); KL(0, 7); MM(0, 3, 2); KH(1, 0); MM(0, 3, 1); KL(1, 0);
      MM(0, 3, 0); UL(1, 7); MM(0, 4, 3); UL(1, 6); MM(0, 4, 2); KH(1, 1); MM(0, 4, 1); KL(1, 1); MM(0, 4, 0); UL(1, 5);
      MM(0, 5, 2); KH(1, 2); MM(0, 5, 1); KL(1, 2); MM(0, 5, 0); UL(1, 4); MM(0, 6, 1); MM(0, 6, 0); KH(1, 3); MM(0, 7, 0); KL(1, 3);
      MM(1, 0, 7); UL(1, 3); MM(1, 0, 6); MM(1, 1, 6); MM(1, 0, 5); KH(1, 4); MM(1, 1, 5); KL(1, 4); MM(1, 2, 5); UL(1, 2); MM(1, 0, 4);
      MM(1, 1, 4); MM(1, 2, 4); MM(1, 3, 4); KH(1, 5); MM(1, 0, 3); KL(1, 5); MM(1, 1, 3); UL(1, 1); MM(1, 2, 3); MM(1, 3, 3);
      MM(1, 4, 3); MM(1, 0, 2); MM(1, 1, 2); KH(1, 6); MM(1, 2, 2); KL(1, 6); MM(1, 3, 2); UL(1, 0); MM(1, 4, 2); MM(1, 5, 2);
      MM(1, 0, 1); KH(1, 7); MM(1, 1, 1); KL(1, 7); MM(1, 2, 1); KH(2, 0); MM(1, 3, 1); KL(2, 0); MM(1, 4, 1); UL(2, 7);
      MM(1, 5, 1); UL(2, 6); MM(1, 6, 1); UL(2, 5); MM(1, 0, 0); UL(2, 4); MM(1, 1, 0); UL(2, 3); MM(1, 2, 0); UL(2, 2);
      MM(1, 3, 0); UL(2, 1); MM(1, 4, 0); UL(2, 0); MM(1, 5, 0); KH(2, 1); MM(1, 6, 0); KL(2, 1); MM(1, 7, 0); MM(2, 0, 7); MM(2, 0, 6);
      MM(2, 0, 5); MM(2, 0, 4); MM(2, 0, 3); KH(2, 2); MM(2, 0, 2); KL(2, 2); MM(2, 0, 1); MM(2, 0, 0); MM(2, 1, 6); MM(2, 1, 5);
      MM(2, 1, 4); KH(2, 3); MM(2, 1, 3); KL(2, 3); MM(2, 1, 2); MM(2, 1, 1); MM(2, 1, 0); MM(2, 2, 5); KH(2, 4); MM(2, 2, 4); KL(2, 4);
      MM(2, 2, 3); KH(2, 5); MM(2, 2, 2); KL(2, 5); MM(2, 2, 1); KH(2, 6); MM(2, 2, 0); KL(2, 6); MM(2, 3, 4); KH(2, 7);
      MM(2, 3, 3); KL(2, 7); MM(2, 3, 2); KH(3, 0); MM(2, 3, 1); KL(3, 0); MM(2, 3, 0); UL(3, 7); MM(2, 4, 3); UL(3, 6);
      MM(2, 4, 2); KH(3, 1); MM(2, 4, 1); KL(3, 1); MM(2, 4, 0); UL(3, 5); MM(2, 5, 2); KH(3, 2); MM(2, 5, 1); KL(3, 2);
      MM(2, 5, 0); UL(3, 4); MM(2, 6, 1); MM(2, 6, 0); KH(3, 3); MM(2, 7, 0); KL(3, 3); MM(3, 0, 7); UL(3, 3); MM(3, 0, 6); MM(3, 1, 6);
      MM(3, 0, 5); KH(3, 4); MM(3, 1, 5); KL(3, 4); MM(3, 2, 5); UL(3, 2); MM(3, 0, 4); MM(3, 1, 4); MM(3, 2, 4); MM(3, 3, 4); KH(3, 5);
      MM(3, 0, 3); KL(3, 5); MM(3, 1, 3); UL(3, 1); MM(3, 2, 3); MM(3, 3, 3); MM(3, 4, 3); MM(3, 0, 2); MM(3, 1, 2); KH(3, 6);
      MM(3, 2, 2); KL(3, 6); MM(3, 3, 2); UL(3, 0); MM(3, 4, 2); MM(3, 5, 2); MM(3, 0, 1); MM(3, 1, 1); MM(3, 2, 1);
      MM(3, 3, 1); KH(3, 7); MM(3, 4, 1); KL(3, 7); set_prog(myprog, 6); MM(3, 5, 1); MM(3, 6, 1); MM(3, 0, 0); MM(3, 1, 0);
      MM(3, 2, 0); MM(3, 3, 0); MM(3, 4, 0); MM(3, 5, 0); MM(3, 6, 0); MM(3, 7, 0);
#undef KL
#undef KH
#undef UL
#undef MM
      SCHED();
    }
    __builtin_amdgcn_s_setprio(3);
    TR(6);
    wait_prog(xprog, 6, cX);
    {
      f4 yk[4];
      if (mh == 0) {
#pragma unroll
        for (int j = 0; j < 4; j++) { lds_st<f4>(scr + j * 1024 + lane * 16, ya[4 + j]); yk[j] = ya[j]; }
      } else {
#pragma unroll
        for (int j = 0; j < 4; j++) { lds_st<f4>(scr + j * 1024 + lane * 16, ya[j]); yk[j] = ya[4 + j]; }
      }
      set_prog(myprog, 7);
      wait_prog(xprog, 7, cX, pmask);
#pragma unroll
      for (int j = 0; j < 4; j++) yk[j] += lds_ld<f4>(oscr + j * 1024 + lane * 16);
      set_prog(myprog, 8);
#pragma unroll
      for (int z = 0; z < 2; z++) {
        const auto r0 = __builtin_amdgcn_permlane16_swap(pkbf(yk[2 * z][0], yk[2 * z][1]), pkbf(yk[2 * z + 1][0], yk[2 * z + 1][1]), false, false);
        const auto r1 = __builtin_amdgcn_permlane16_swap(pkbf(yk[2 * z][2], yk[2 * z][3]), pkbf(yk[2 * z + 1][2], yk[2 * z + 1][3]), false, false);
        const unsigned e0 = r0[0], e1 = r1[0], e2 = r0[1], e3 = r1[1];
        st128(rs, v4u{e0, e1, e2, e3}, WS_YT + (256 * (2 * z + (g & 1)) + 16 * n16 + 8 * (g >> 1)) * 2, ch * 4096 + mh * 2048);
      }
      TR(13);
      WAITV(0);
      st32(rs, tag, lane == 0 ? 0 : OOB, WS_YFLAG + (((ch >> 6) * 2 + mh) * 64 + (ch & 63)) * 4);
    }
    TR(7);
    wait_prog(bprog, YGATE, cB);
    TR(14);
    {
      const int yo = WS_YFLAG + ((cb * 2 + (tb >> 4)) * 64 + 16 * rw + n16) * 4;
      if (!wall(ld32(rs, yo, 0) == tag)) { wait_prog(bprog, 13, cB); poll_tag(rs, yo, tag); }
      CFENCE();
    }
    TR(15);
    {
#pragma unroll
      for (int j = 0; j < 2; j++) {
        const int row = 16 * rw + 8 * j + (lane >> 3);
        const v4u y = ld128(rs, WS_YT + row * 4096 + (lane & 7) * 16, cb * 64 * 4096 + tb * 128);
        lds_st<v4u>(lds0 + LD_UT + row * 128 + (((lane & 7) ^ (((lane >> 4) & 1) << 2)) << 4), y);
      }
      set_prog(myprog, 9);
    }
    ROLE_END();
  } else {
    const bool isA = role == 3;
    if (isA) __builtin_amdgcn_s_setprio(PRIO_A); else __builtin_amdgcn_s_setprio(3);
    const unsigned gprog = isA ? aprog : bprog, myprog = gprog + rw, pmask = 0x80u << (8 * (rw ^ 1));
    const int th = rw >> 1, kh = rw & 1;
    const int tbk = isA ? tb ^ 1 : tb;
    unsigned cG = 0x80808080u, tag = 0;
    v2u xres[4];
#pragma unroll
    for (int j = 0; j < 4; j++) xres[j] = v2u{0u, 0u};
    if (isA) {
      START_BARRIER();
      if (!has1) set_prog(myprog, 9);
    } else {
      const int st0 = 64 * tb + 8 * cb + 2 * rw;
      START_BARRIER();
      unsigned tg;
      asm volatile("s_load_dword %0, %1, 0x0\n\ts_waitcnt lgkmcnt(0)" : "=s"(tg) : "s"(ws) : "memory");
      tg += 1u;
      tag = tg;
      lds_st<unsigned>(lds0 + LD_TAG, tg);
      asm volatile("s_waitcnt vmcnt(0)" : "+v"(i0), "+v"(i1), "+v"(i2), "+v"(i3)::"memory");
      const v4u r0 = i0, r1 = i1, t0 = i2, t1 = i3;
      const unsigned scr = lds0 + 7 * 16384 + rw * 2048;
      lds_st<v4u>(scr + lane * 16, t0);
      lds_st<v4u>(scr + 1024 + lane * 16, t1);
      CFENCE();
      v4u tr[2];
#pragma unroll
      for (int j = 0; j < 2; j++) {
        const unsigned a = scr + (8 * g + q) * 64 + 32 * j + 8 * pp;
        tr[j] = __builtin_bit_cast(v4u, cat(tr_ld(a), tr_ld(a + 256)));
      }
#pragma unroll
      for (int j = 0; j < 4; j++) xres[j] = lds_ld<v2u>(scr + n32 * 64 + 16 * j + 8 * hh);
      set_prog(myprog, has1 ? 1 : 9);
      TR(1);
#pragma unroll
      for (int j = 0; j < 2; j++) {
        const int cho = 64 * cb + 32 * kh + 16 * j + n16;
#pragma unroll
        for (int hf2 = 0; hf2 < 2; hf2++)
          st128(rs, v4u{tr[j][2 * hf2], tg, tr[j][2 * hf2 + 1], tg}, (cho >> 1) * 16384 + (cho & 1) * 64 + g * 16, WS_XT + hf2 * 8192 + (2 * tb + th) * 128);
      }
      float s1 = 0.f, s2 = 0.f;
#pragma unroll
      for (int j = 0; j < 4; j++) {
        s1 = dot2(r1[j], 0x3f803f80u, dot2(r0[j], 0x3f803f80u, s1));
        s2 = dot2(r1[j], r1[j], dot2(r0[j], r0[j], s2));
      }
      const float m = add_x16(sum16(s1)) * (1.f / D_);
      const float var = rbf(__builtin_fmaxf(add_x16(sum16(s2)) * (1.f / D_) - m * m, 0.f));
      const unsigned gv = pkbf(m, __builtin_amdgcn_sqrtf(rbf(var + 1e-5f)));
      const int sbk = st0 >> 3;
      st64(rs, v2u{gv, tg}, n32 == 0 ? hh * 8 : OOB, WS_GRAN + (((sbk >> 6) * 4 + rw) * 64 + (sbk & 63)) * 16);
      __builtin_amdgcn_s_setprio(PRIO_A);
    }
    TR(2);
    constexpr int WIN = 3;
    v4u hf[WIN][4];
    const int hb = WS_H + ((2 * tbk + th) * 112 + 56 * fh + 4 * kh) * 1024;
    const int hfl = WS_HFLAG + ((tbk * 2 + fh) * 4 + rw) * 1024;
    if (has1) {
      f16v acc[2];
#pragma unroll
      for (int i = 0; i < 16; i++) acc[0][i] = acc[1][i] = 0.f;
      const Geo G = {lds0, lane, rw, hh, blk, q, pp, g, n16, n32};
      const int t = 64 * tbk + 32 * th + n32;
      const int mo = WS_GRAN + ((((t >> 9) * 4 + ((t >> 1) & 3)) * 64 + ((t >> 3) & 63)) * 2 + (t & 1)) * 8;
      const int co = WS_CSUM + (slice * 128 + 64 * kh + lane) * 8;
      v2u gm, gc;
      if (isA) {
        gemm1_loop<true>(G, acc, cL, [&](int s) { if (s >= 8) TR(s == 16 ? 3 : s - 4); if (s == 5) { gm = ld64(rs, mo, 0); gc = ld64(rs, co, 0); tag = lds_ld<unsigned>(lds0 + LD_TAG); } });
      } else {
        gemm1_loop<false>(G, acc, cL, [&](int s) {
          if (s >= 8) TR(s == 16 ? 3 : s - 4);
          if (s == 3) { gm = ld64(rs, mo, 0); gc = ld64(rs, co, 0); __builtin_amdgcn_s_setprio(PRIO_B); }
        });
      }

      if (!wall(gm.y == tag && gc.y == tag)) {
#pragma unroll 1
        for (unsigned i = 0; i < kMemSpin; ++i) {
          CFENCE();
          gm = ld64(rs, mo, 0); gc = ld64(rs, co, 0);
          if (wall(gm.y == tag && gc.y == tag)) break;
          __builtin_amdgcn_s_sleep(4);
        }
      }
      {
        const unsigned m = (gm.x ^ 0x8000u) & 0xffffu;
        const s8 cm = __builtin_bit_cast(s8, v4u{m | (m << 16), m, 0u, 0u});
        const float cs = u2f(gc.x), c0 = rbf(cs), c1 = rbf(cs - c0);
        const unsigned p0 = pkbf(c0, c1), p1 = pkbf(cs - c0 - c1, 0.f);
        acc[0] = mfma32(__builtin_bit_cast(s8, v4u{hh ? 0u : p0, hh ? 0u : p1, 0u, 0u}), cm, acc[0]);
        acc[1] = mfma32(__builtin_bit_cast(s8, v4u{hh ? p0 : 0u, hh ? p1 : 0u, 0u, 0u}), cm, acc[1]);
      }
      TR(12);
#pragma unroll
      for (int s = 0; s < 4; s++) {
#pragma unroll
        for (int j = 0; j < 4; j++) {
          const unsigned p = pkbf(acc[s >> 1][8 * (s & 1) + 2 * j], acc[s >> 1][8 * (s & 1) + 2 * j + 1]);
          hf[0][s][j] = __builtin_bit_cast(unsigned, __builtin_elementwise_max(__builtin_bit_cast(sh2, p), sh2{0, 0}));
        }
        st128(rs, hf[0][s], lane * 16 + s * 1024, hb + cb * 8192);
      } TR(13);
      WAITV(0);
      TR(14);
      st32(rs, tag, lane == 0 ? 0 : OOB, hfl + cb * 128);
    } else if (isA) {
      wait_prog(bprog, 1, cB);
      tag = lds_ld<unsigned>(lds0 + LD_TAG);
    }
    wait_prog(lprog, 11, cL);
    if (!isA) { __builtin_amdgcn_s_setprio(PRIO_B); wait_prog(aprog, 12, cA); set_prog(myprog, 11); }
    TR(15);
    poll_tag(rs, hfl + umin(lane, 6) * 128, tag);
    TR(16);
    TR(17);
    auto ldh = [&](int c, int s) {
      int sg = c + cb; sg = sg >= 7 ? sg - 7 : sg;
      hf[c % WIN][s] = ld128(rs, lane * 16 + s * 1024, hb + sg * 8192);
    };
    if (!has1) {
#pragma unroll
      for (int s = 0; s < 4; s++) ldh(0, s);
    }
#pragma unroll
    for (int c = 1; c < WIN; c++)
#pragma unroll
      for (int s = 0; s < 4; s++) { ldh(c, s); SCHED(); }
    set_prog(myprog, 12);
    unsigned a2[2];
    int un = cb + 7 - 2 * pl; un = un >= 7 ? un - 7 : un; un = un >= 7 ? un - 7 : un;
#pragma unroll
    for (int ct = 0; ct < 2; ct++) a2[ct] = lds0 + un * 16384 + (64 * kh + 4 * hh + q) * 128 + ((ct ^ (q >> 1)) << 6) + 32 * blk + 8 * pp;
    constexpr int PD2 = 2;
    s8 fw2[PD2 + 1][2];
    auto rd2 = [&](int i) {
      if (i > 0 && (i & 3) == 0) {
        const int d = un == 6 ? -6 * 16384 : 16384;
        un = un == 6 ? 0 : un + 1;
        a2[0] += d; a2[1] += d;
      }
#pragma unroll
      for (int ct = 0; ct < 2; ct++) {
        const unsigned a = a2[ct] + (i & 3) * 2048;
        fw2[i % (PD2 + 1)][ct] = cat(tr_ld(a), tr_ld(a + 1024));
      }
    };
    wait_prog(lprog, 15, cL);
#pragma unroll
    for (int i = 0; i < PD2; i++) rd2(i);
    f16v ac2[2];
#pragma unroll
    for (int i = 0; i < 16; i++) ac2[0][i] = ac2[1][i] = 0.f;
#pragma unroll
    for (int i = 0; i < 28; i++) {
      const int c = i >> 2, s = i & 3;
      SCHED();
      ac2[0] = mfma32(fw2[i % (PD2 + 1)][0], __builtin_bit_cast(s8, hf[c % WIN][s]), ac2[0]);
      ac2[1] = mfma32(fw2[i % (PD2 + 1)][1], __builtin_bit_cast(s8, hf[c % WIN][s]), ac2[1]);
      SCHED();
      if (i + PD2 < 28) rd2(i + PD2);
      if (c + WIN < 7) ldh(c + WIN, s);
      if (s == 3) TR(18 + c);
      if (i == 15) set_prog(myprog, 13);
    }
    SCHED();
    if (!isA) wait_prog(xprog, 8, cX);
    const unsigned ex = isA ? lds0 + 7 * 16384 : lds0 + LD_KT;
    f16v keep;
    if (kh == 0) {
#pragma unroll
      for (int j = 0; j < 4; j++) lds_st<f4>(ex + rw * 4096 + j * 1024 + lane * 16, f4{ac2[1][4 * j], ac2[1][4 * j + 1], ac2[1][4 * j + 2], ac2[1][4 * j + 3]});
      keep = ac2[0];
    } else {
#pragma unroll
      for (int j = 0; j < 4; j++) lds_st<f4>(ex + rw * 4096 + j * 1024 + lane * 16, f4{ac2[0][4 * j], ac2[0][4 * j + 1], ac2[0][4 * j + 2], ac2[0][4 * j + 3]});
      keep = ac2[1];
    }
    set_prog(myprog, 14);
    TR(25);
    wait_prog(gprog, 14, cG, pmask);
#pragma unroll
    for (int j = 0; j < 4; j++) {
      const f4 o = lds_ld<f4>(ex + (rw ^ 1) * 4096 + j * 1024 + lane * 16);
#pragma unroll
      for (int e = 0; e < 4; e++) keep[4 * j + e] += o[e];
    }
    const unsigned bx = lds0 + 7 * 16384 + (rw ^ 1) * 4096 + lane * 16;
    if (isA) {
#pragma unroll
      for (int j = 0; j < 4; j++)
        st128(rs, v4u{pkbf(keep[4 * j], keep[4 * j + 1]), tag, pkbf(keep[4 * j + 2], keep[4 * j + 3]), tag}, WS_PART + lane * 16 + j * 1024, (b * 4 + rw) * 4096); TR(26);
      wait_prog(bprog, 13, cB);
      TR(27);
      v4u pr[4];
#pragma unroll 1
      for (unsigned i = 0; i < kMemSpin; ++i) {
        CFENCE();
#pragma unroll
        for (int j = 0; j < 4; j++) pr[j] = ld128(rs, WS_PART + lane * 16 + j * 1024, ((b ^ 1) * 4 + rw) * 4096);
        CFENCE();
        unsigned bad = 0;
#pragma unroll
        for (int j = 0; j < 4; j++) bad |= (pr[j].y ^ tag) | (pr[j].w ^ tag);
        if (wall(bad == 0)) break;
        __builtin_amdgcn_s_sleep(2);
      }
      CFENCE();
      TR(28);
      wait_prog(xprog, 9, cX);
      const float rstd = lds_ld<float>(lds0 + LD_RS + (32 * th + n32) * 4);
      const unsigned ay = lds0 + LD_UT + (32 * kh + 4 * hh + q) * 128 + ((th ^ (q >> 1)) << 6) + 32 * blk + 8 * pp;
#pragma unroll
      for (int j = 0; j < 4; j++) {
        const v2u y = __builtin_bit_cast(v2u, tr_ld(ay + j * 1024));
        lds_st<f4>(bx + j * 1024, f4{bflo(y.x) + rstd * bflo(pr[j].x), bfhi(y.x) + rstd * bfhi(pr[j].x),
                                     bflo(y.y) + rstd * bflo(pr[j].z), bfhi(y.y) + rstd * bfhi(pr[j].z)});
      }
      set_prog(myprog, 15);
      TR(29);
      ROLE_END();
    } else {
      const float rstd = lds_ld<float>(lds0 + LD_RS + (32 * th + n32) * 4);
#pragma unroll
      for (int j = 0; j < 4; j++) {
        keep[4 * j] = bflo(xres[j].x) + rstd * keep[4 * j]; keep[4 * j + 1] = bfhi(xres[j].x) + rstd * keep[4 * j + 1];
        keep[4 * j + 2] = bflo(xres[j].y) + rstd * keep[4 * j + 2]; keep[4 * j + 3] = bfhi(xres[j].y) + rstd * keep[4 * j + 3];
      } TR(26);
      wait_prog(aprog, 15, cA, 0x80u << (8 * rw));
      TR(27);
      v2u pk[4];
#pragma unroll
      for (int j = 0; j < 4; j++) {
        const f4 o = lds_ld<f4>(bx + j * 1024);
        pk[j].x = pkbf(keep[4 * j] + o[0], keep[4 * j + 1] + o[1]);
        pk[j].y = pkbf(keep[4 * j + 2] + o[2], keep[4 * j + 3] + o[3]);
      }
      u16* op = out + (size_t)(64 * tb + 32 * th + n32) * D_ + 64 * cb + 32 * kh + 8 * hh;
#pragma unroll
      for (int j = 0; j < 4; j += 2) {
        const auto r0 = __builtin_amdgcn_permlane32_swap(pk[j].x, pk[j + 1].x, false, false);
        const auto r1 = __builtin_amdgcn_permlane32_swap(pk[j].y, pk[j + 1].y, false, false);
        const unsigned e0 = r0[0], e1 = r1[0], e2 = r0[1], e3 = r1[1];
        *(v4u*)(op + 8 * j) = v4u{e0, e1, e2, e3};
      }
      TR(29);
      ROLE_END();
    }
  }
}

}
#if defined(__HIP_DEVICE_COMPILE__)
#pragma clang attribute pop
#endif

extern "C" void kernel_launch(void* const* d_in, const int* in_sizes, int n_in, void* d_out, int out_size, void* d_ws,
                              size_t ws_size, hipStream_t stream) {
  (void)in_sizes; (void)n_in; (void)out_size; (void)ws_size;
  (void)hipFuncSetAttribute((const void*)fused_kernel, hipFuncAttributeMaxDynamicSharedMemorySize, LDS_BYTES);
  fused_kernel<<<256, 1024, LDS_BYTES, stream>>>((const u16*)d_in[0], (const u16*)d_in[1], (const u16*)d_in[2], (const u16*)d_in[3],
                                                (const u16*)d_in[4], (const u16*)d_in[5], (const u16*)d_in[6], (const u16*)d_in[8],
                                                (const u16*)d_in[9], (u16*)d_out, (char*)d_ws);
}
```

```cpp
#include <hip/hip_runtime.h>
#include <stdint.h>

#if defined(__HIP_DEVICE_COMPILE__)
#pragma clang attribute push(__attribute__((target("no-packed-fp32-ops"))), apply_to = function)
#endif
namespace {

typedef unsigned short u16;
typedef unsigned long long u64;
typedef short s4 __attribute__((ext_vector_type(4)));
typedef short s8 __attribute__((ext_vector_type(8)));
typedef short sh2 __attribute__((ext_vector_type(2)));
typedef __bf16 b2 __attribute__((ext_vector_type(2)));
typedef _Float16 h2 __attribute__((ext_vector_type(2)));
typedef _Float16 h8 __attribute__((ext_vector_type(8)));
typedef float f2 __attribute__((ext_vector_type(2)));
typedef float f4 __attribute__((ext_vector_type(4)));
typedef float f16v __attribute__((ext_vector_type(16)));
typedef unsigned v2u __attribute__((ext_vector_type(2)));
typedef unsigned v4u __attribute__((ext_vector_type(4)));
#define LAS __attribute__((address_space(3)))
#define DI __device__ __forceinline__

#define PRIO_L 2
#define PRIO_L2 1
#define PRIO_A 2
#define PRIO_B 1
#define PRIO_X 2
#define PRIO_X2 0
#define PSLEEP 12
#define W2POS 4
#define BGATE 12
#define IGATE 2
#define WARM 0
#define WSMASK 15
#define XGATE 2
#define YGATE 13
constexpr int D_ = 512, F_ = 1792, N_ = 128;
#define CODE_LINES 208

constexpr int WS_EPOCH = 0;
constexpr int WS_YFLAG_UNUSED = 4096;
constexpr int WS_GRAN = 16384;
constexpr int WS_CSUM = 32768;
constexpr int WS_HFLAG = 65536;
constexpr int WS_XT = WS_HFLAG + 2048 * 128;
constexpr int WS_YT = WS_XT + 4 * 1024 * 1024;
constexpr int WS_PART = WS_YT + 4 * 1024 * 1024;
constexpr int WS_H = WS_PART + 4 * 1024 * 1024;
constexpr int WS_END = WS_H + 64 * 112 * 1024;
constexpr int WS_STAMP = 32 * 1024 * 1024;

constexpr int LD_KT = 131072;
constexpr int KT_COPY = 5120 + 64;
constexpr int KT_CH = 2 * KT_COPY;
constexpr int LD_UT = LD_KT + 2 * KT_CH;
constexpr int UT_CH = 4576;
constexpr int LD_CTL = LD_UT + 2 * UT_CH;
constexpr int LD_APROG = LD_CTL, LD_BPROG = LD_CTL + 4, LD_LPROG = LD_CTL + 8, LD_XPROG = LD_CTL + 12, LD_TAG = LD_CTL + 16;
constexpr int LD_RS = LD_CTL + 64;
constexpr int LD_DUMP = LD_RS + 256;
constexpr int LD_END = LD_DUMP + 1024;
constexpr int LDS_BYTES = 163840;
static_assert(LD_END <= LDS_BYTES, "lds");
static_assert(LD_UT % 16 == 0 && LD_CTL % 16 == 0 && KT_COPY % 16 == 0, "align");

constexpr unsigned kLdsSpin = 1u << 18, kMemSpin = 1u << 13;

DI float u2f(unsigned v) { return __builtin_bit_cast(float, v); }
DI unsigned f2u(float v) { return __builtin_bit_cast(unsigned, v); }
DI float bflo(unsigned v) { return u2f(v << 16); }
DI float bfhi(unsigned v) { return u2f(v & 0xffff0000u); }
DI unsigned pkbf(float a, float b) { f2 v = {a, b}; return __builtin_bit_cast(unsigned, __builtin_convertvector(v, b2)); }
DI unsigned pkh(float a, float b) { f2 v = {a, b}; return __builtin_bit_cast(unsigned, __builtin_convertvector(v, h2)); }
DI float rbf(float a) { return bflo(pkbf(a, a)); }
DI float dot2(unsigned a, unsigned b, float acc) {
  return __builtin_amdgcn_fdot2_f32_bf16(__builtin_bit_cast(b2, a), __builtin_bit_cast(b2, b), acc, false);
}
DI unsigned umin(unsigned a, unsigned b) { return a < b ? a : b; }
DI unsigned umax(unsigned a, unsigned b) { return a > b ? a : b; }
DI bool wall(bool p) { return __builtin_amdgcn_ballot_w64(!p) == 0; }
template <class T> DI T lds_ld(unsigned a) { return *(LAS T*)(a); }
template <class T> DI void lds_st(unsigned a, T v) { *(LAS T*)(a) = v; }
DI s4 tr_ld(unsigned a) { return __builtin_amdgcn_ds_read_tr16_b64_v4i16((LAS s4*)(a)); }
DI s8 cat(s4 a, s4 b) { return __builtin_shufflevector(a, b, 0, 1, 2, 3, 4, 5, 6, 7); }
DI f16v mfma32(s8 a, s8 b, f16v c) { return __builtin_amdgcn_mfma_f32_32x32x16_bf16(a, b, c, 0, 0, 0); }
DI f4 mfma16(s8 a, s8 b, f4 c) { return __builtin_amdgcn_mfma_f32_16x16x32_bf16(a, b, c, 0, 0, 0); }
DI f4 mfma16h(v4u a, v4u b, f4 c) {
  return __builtin_amdgcn_mfma_f32_16x16x32_f16(__builtin_bit_cast(h8, a), __builtin_bit_cast(h8, b), c, 0, 0, 0);
}
#define SCHED() __builtin_amdgcn_sched_barrier(0)
#define CFENCE() asm volatile("" ::: "memory")
#define WAITV(n) asm volatile("s_waitcnt vmcnt(" #n ")" ::: "memory")

#define SPIN_SLEEP
DI void set_prog(unsigned addr, unsigned v) { asm volatile("ds_write_b8 %0, %1" ::"v"(addr), "v"(v | 0x80u) : "memory"); }
DI void wait_prog(unsigned addr, unsigned need, unsigned& cache, unsigned mask = 0x80808080u) {
  unsigned t, cnt;
  cache = __builtin_amdgcn_readfirstlane(cache);
  asm volatile(
      "s_mov_b32 %[cnt], %[lim]\n"
      "1:\n\t"
      "s_sub_u32 %[t], %[c], %[n]\n\t"
      "s_andn2_b32 %[t], %[m], %[t]\n\t"
      "s_cbranch_scc0 2f\n\t"
      SPIN_SLEEP
      "ds_read_b32 v127, %[a]\n\t"
      "s_waitcnt lgkmcnt(0)\n\t"
      "v_readfirstlane_b32 %[c], v127\n\t"
      "s_sub_u32 %[cnt], %[cnt], 1\n\t"
      "s_cbranch_scc0 1b\n"
      "2:"
      : [c] "+&s"(cache), [t] "=&s"(t), [cnt] "=&s"(cnt)
      : [a] "v"(addr), [n] "s"(need * 0x01010101u), [m] "s"(mask), [lim] "i"(kLdsSpin)
      : "memory", "scc", "v127");
}
DI bool prog_ok(unsigned cache, unsigned need) { return (0x80808080u & ~(cache - need * 0x01010101u)) == 0; }
DI void glds(unsigned voff, const char* sbase, unsigned m0v) {
  asm volatile("s_mov_b32 m0, %2\n\ts_nop 0\n\tglobal_load_lds_dwordx4 %0, %1" ::"v"(voff), "s"(sbase), "s"(m0v) : "memory");
}
constexpr int SC1 = 16;
constexpr int OOB = 0x7ffffff0;
typedef __amdgpu_buffer_rsrc_t rsrc_t;
DI v4u ld128(rsrc_t r, int voff, int soff) { return __builtin_amdgcn_raw_buffer_load_b128(r, voff, soff, SC1); }
DI v2u ld64(rsrc_t r, int voff, int soff) { return __builtin_amdgcn_raw_buffer_load_b64(r, voff, soff, SC1); }
DI unsigned ld32(rsrc_t r, int voff, int soff) { return __builtin_amdgcn_raw_buffer_load_b32(r, voff, soff, SC1); }
DI void st128(rsrc_t r, v4u v, int voff, int soff) {
  __builtin_amdgcn_raw_buffer_store_b128(v, r, voff, soff, SC1);
  asm volatile("s_nop 1" ::"v"(v) : "memory");
}
DI void st64(rsrc_t r, v2u v, int voff, int soff) { __builtin_amdgcn_raw_buffer_store_b64(v, r, voff, soff, SC1); }
DI void st32(rsrc_t r, unsigned v, int voff, int soff) { __builtin_amdgcn_raw_buffer_store_b32(v, r, voff, soff, SC1); }
DI v4u ldp128(rsrc_t r, int voff, int soff) { return __builtin_amdgcn_raw_buffer_load_b128(r, voff, soff, 0); }
DI void poll_tag(rsrc_t r, int voff, unsigned tag) {
#pragma unroll 1
  for (unsigned i = 0; i < kMemSpin; ++i) {
    CFENCE();
    const unsigned v = ld32(r, voff, 0);
    if (wall(v == tag)) break;
    __builtin_amdgcn_s_sleep(PSLEEP);
  }
  CFENCE();
}
template <int CTRL> DI float dpp(float v) { return u2f(__builtin_amdgcn_update_dpp(0u, f2u(v), CTRL, 0xf, 0xf, true)); }
DI float sum16(float v) { v += dpp<0xB1>(v); v += dpp<0x4E>(v); v += dpp<0x141>(v); return v + dpp<0x140>(v); }
DI float add_x16(float v) { const auto r = __builtin_amdgcn_permlane16_swap(f2u(v), f2u(v), false, false); const unsigned a = r[0], b = r[1]; return u2f(a) + u2f(b); }
DI float add_x32(float v) { const auto r = __builtin_amdgcn_permlane32_swap(f2u(v), f2u(v), false, false); const unsigned a = r[0], b = r[1]; return u2f(a) + u2f(b); }
template <int CTRL> DI unsigned dppmax(unsigned v) { return umax(v, __builtin_amdgcn_update_dpp(0u, v, CTRL, 0xf, 0xf, true)); }
DI unsigned wave_umax(unsigned v) {
  v = dppmax<0x140>(dppmax<0x141>(dppmax<0x4E>(dppmax<0xB1>(v))));
  return umax(umax(__builtin_amdgcn_readlane(v, 0), __builtin_amdgcn_readlane(v, 16)), umax(__builtin_amdgcn_readlane(v, 32), __builtin_amdgcn_readlane(v, 48)));
}
struct cpx { float r, i; };
DI cpx cmul(cpx a, cpx b) { return {a.r * b.r - a.i * b.i, a.r * b.i + a.i * b.r}; }
DI cpx cexp_at(float lr, float rev, float p) {
  const float e = __builtin_amdgcn_exp2f(lr * p);
  const float f = __builtin_amdgcn_fractf(rev * p);
  return {e * __builtin_amdgcn_cosf(f), e * __builtin_amdgcn_sinf(f)};
}
constexpr float kLog2e = 1.44269504088896341f;

#define TR(i)
struct Geo {
  unsigned lds0; int lane, rw, hh, blk, q, pp, g, n16, n32;
};

template <bool IS_A, class F>
DI void gemm1_loop(const Geo& G, f16v (&acc)[2], unsigned& cL, F&& hook) {
  const int th = G.rw >> 1, kh = G.rw & 1;
  const unsigned lprog = G.lds0 + LD_LPROG, myprog = G.lds0 + (IS_A ? LD_APROG : LD_BPROG) + G.rw;
  unsigned ax[4], aw[2];
#pragma unroll
  for (int s = 0; s < 4; s++) ax[s] = G.lds0 + ((IS_A ? 0 : 64) + 32 * th + G.n32) * 128 + (((2 * s + G.hh) ^ ((G.n32 >> 1) & 7)) << 4);
#pragma unroll
  for (int ft = 0; ft < 2; ft++) aw[ft] = G.lds0 + 16384 + (8 * G.hh + G.q) * 256 + (((2 * kh + ft) ^ G.q) << 6) + 32 * G.blk + 8 * G.pp;
  constexpr int PD = 2;
  s8 fx[PD + 1], fw[PD + 1][2];
  auto rdx = [&](int i) { fx[i % (PD + 1)] = lds_ld<s8>(ax[i & 3] + ((i >> 2) & 1) * 32768); };
  auto rdw = [&](int i, int ft) {
    const unsigned a = aw[ft] + ((i >> 2) & 1) * 32768 + (i & 3) * 4096;
    fw[i % (PD + 1)][ft] = cat(tr_ld(a), tr_ld(a + 1024));
  };
  wait_prog(lprog, 1, cL);
  hook(16);
#pragma unroll
  for (int i = 0; i < PD; i++) { rdx(i); rdw(i, 0); rdw(i, 1); }
  unsigned pv = 0;
#pragma unroll
  for (int i = 0; i < 32; i++) {
    const bool p = i + PD < 32;
    SCHED();
    if ((i & 3) == 2) {
      const int s = i >> 2;
      if (p) cL = __builtin_amdgcn_readfirstlane(pv);
      SCHED();
      set_prog(myprog, s + 2);
      hook(s);
      if (p) wait_prog(lprog, (IS_A || s < 3) ? s + 2 : (s < 5 ? 9 : 10), cL);
      hook(s + 8);
      if (p && (s & 1)) {
#pragma unroll
        for (int t = 0; t < 4; t++) ax[t] ^= 65536;
        aw[0] ^= 65536; aw[1] ^= 65536;
      }
      SCHED();
    }
    acc[0] = mfma32(fw[i % (PD + 1)][0], fx[i % (PD + 1)], acc[0]);
    SCHED();
    if ((i & 3) == 0 && i < 28) pv = *(volatile LAS unsigned*)lprog;
    if (p) { rdx(i + PD); rdw(i + PD, 0); }
    SCHED();
    acc[1] = mfma32(fw[i % (PD + 1)][1], fx[i % (PD + 1)], acc[1]);
    SCHED();
    if (p) rdw(i + PD, 1);
  }
  SCHED();
}

__global__ __launch_bounds__(1024, 4) void fused_kernel(
    const u16* __restrict__ x, const u16* __restrict__ logA, const u16* __restrict__ Aim, const u16* __restrict__ Cre,
    const u16* __restrict__ Cim, const u16* __restrict__ logdt, const u16* __restrict__ Dsk, const u16* __restrict__ w1,
    const u16* __restrict__ w2, u16* __restrict__ out, char* ws) {
  extern __shared__ __attribute__((aligned(16))) char smem[];
  u64 pc0;
  asm volatile("s_getpc_b64 %0" : "=s"(pc0));
  const unsigned lds0 = (unsigned)(size_t)(LAS char*)smem;
  const int tid = __builtin_amdgcn_workitem_id_x(), lane = tid & 63;
  const int w = __builtin_amdgcn_readfirstlane(tid >> 6), role = w >> 2, rw = w & 3;
  const int b = __builtin_amdgcn_workgroup_id_x(), xcd = b & 7, idx = b >> 3;
  const int fh = xcd & 1, pl = idx & 3, cb = idx >> 2;
  const bool has1 = cb < 7;
  const int panel = 4 * (xcd >> 1) + pl, slice = 7 * fh + cb;
  const int tb = 2 * panel + fh;
  const rsrc_t rs = __builtin_amdgcn_make_buffer_rsrc(ws, 0, WS_END, 0x00020000);
  const int hh = lane >> 5, blk = (lane >> 4) & 1, q = (lane & 15) >> 2, pp = lane & 3, g = lane >> 4, n16 = lane & 15, n32 = lane & 31;
  const unsigned lprog = lds0 + LD_LPROG, aprog = lds0 + LD_APROG, bprog = lds0 + LD_BPROG, xprog = lds0 + LD_XPROG;
  unsigned cL = 0x80808080u, cA = 0x80808080u, cB = 0x80808080u, cX = 0x80808080u;
  unsigned pA, pI, pCr, pCi, pf;
  v4u i0, i1;
  lds_st<unsigned>(lds0 + LD_CTL + (tid & 7) * 4, (tid & 4) ? 0u : 0x80808080u);
  {
    const unsigned po = (unsigned)((2 * b * N_ + 64 * rw + lane) * 2);
    const unsigned wsl = (WARM & 2) ? lane & WSMASK : 0;
    const unsigned co = rw == 0 ? umin(idx * 8 + (lane & 7), CODE_LINES - 1) * 128
                      : rw == 1 ? ((WARM & 1) ? (unsigned)(896 * fh * 1024 + ((xcd >> 1) * 32 + idx) * 7168 + umin(lane, 55) * 128) : 0u)
                      : rw == 2 ? (wsl * 32 + idx) * (unsigned)(WS_END / (32 * (WSMASK + 1)) & ~127)
                                : ((WARM & 4) ? (unsigned)((64 * tb + (lane & 1) * 32) * 1024 + cb * 128) : 0u);
    const u64 cbase = rw == 0 ? (pc0 & ~127ull) : rw == 1 ? (u64)w2 : rw == 2 ? (u64)ws : (u64)out;
    const unsigned ro = (unsigned)((64 * tb + 8 * cb + 2 * rw + hh) * 1024 + n32 * 16);
    const unsigned to = (unsigned)((64 * tb + 32 * (rw >> 1) + (lane >> 2)) * 1024 + 128 * cb + 64 * (rw & 1) + (lane & 3) * 16);
    const unsigned o0 = role == 1 ? to : ro, o1 = o0 + (role == 1 ? 16384u : 512u);
    asm volatile(
        "s_cmp_eq_u32 %[role], 3\n\t"
        "s_cbranch_scc1 2f\n\t"
        "s_cmp_lg_u32 %[role], 0\n\t"
        "s_cbranch_scc1 1f\n\t"
        "global_load_ushort %[a0], %[po], %[pA]\n\t"
        "global_load_ushort %[a1], %[po], %[pI]\n\t"
        "global_load_ushort %[a2], %[po], %[pCr]\n\t"
        "global_load_ushort %[a3], %[po], %[pCi]\n\t"
        "global_load_dword %[a4], %[co], %[pc]\n\t"
        "s_branch 2f\n"
        "1:\n\t"
        "global_load_dwordx4 %[i0], %[o0], %[x]\n\t"
        "global_load_dwordx4 %[i1], %[o1], %[x]\n"
        "2:"
        : [i0] "=&v"(i0), [i1] "=&v"(i1), [a0] "=&v"(pA), [a1] "=&v"(pI), [a2] "=&v"(pCr), [a3] "=&v"(pCi), [a4] "=&v"(pf)
        : [role] "s"(role), [po] "v"(po), [co] "v"(co), [o0] "v"(o0), [o1] "v"(o1), [x] "s"(x), [pA] "s"(logA), [pI] "s"(Aim),
          [pCr] "s"(Cre), [pCi] "s"(Cim), [pc] "s"(cbase), "s"(w1), "s"(w2), "s"(out), "s"(ws), "s"(logdt), "s"(Dsk)
        : "memory", "scc");
  }
  auto get_tag = [&]() {
    v2u t;
#pragma unroll 1
    for (unsigned i = 0; i < kLdsSpin; ++i) {
      t = *(volatile LAS v2u*)(lds0 + LD_TAG);
      if (__builtin_amdgcn_readfirstlane(t.y)) break;
    }
    return (unsigned)__builtin_amdgcn_readfirstlane(t.x);
  };
#define ROLE_END() do { __builtin_amdgcn_endpgm(); __builtin_unreachable(); } while (0)
#define START_BARRIER() asm volatile("s_waitcnt lgkmcnt(0)\n\ts_barrier" ::: "memory")

  if (__builtin_expect(role == 3, 1)) {
    __builtin_amdgcn_s_setprio(PRIO_L);
    const unsigned myprog = lprog + rw;
    const rsrc_t rw2 = __builtin_amdgcn_make_buffer_rsrc((void*)w2, 0, F_ * D_ * 2, 0x00020000);
    const int wo2 = (896 * fh + 32 * rw + (lane >> 3)) * 1024 + cb * 128 + (lane & 7) * 16;
    v4u wq[7][4];
    auto ldw2 = [&](int u) {
      int sg = u + 2 * pl; sg = sg >= 7 ? sg - 7 : sg;
#pragma unroll
      for (int j = 0; j < 4; j++) wq[u][j] = ldp128(rw2, wo2, sg * 131072 + j * 8192);
      CFENCE();
    };
    if (__builtin_expect(has1, 1)) {
      const int rot1 = (cb + 2 * pl) & 7;
      unsigned xo[2];
#pragma unroll
      for (int j = 0; j < 2; j++) xo[j] = (unsigned)((8 * j + (lane >> 3)) * 1024 + (((lane & 7) ^ ((4 * j + g) & 7)) << 4));
      const unsigned wo = (unsigned)(g * 3584 + ((n16 ^ (g << 2)) << 4));
      const char* xa = (const char*)x + (size_t)((tb ^ 1) * 64 + 16 * rw) * 1024;
      const char* xb = (const char*)x + (size_t)(tb * 64 + 16 * rw) * 1024;
      const char* wb = (const char*)w1 + (size_t)(16 * rw) * 3584 + slice * 256;
      auto issue_x = [&](int s, int grp) {
        const int ss = (s + rot1) & 7;
#pragma unroll
        for (int j = 0; j < 2; j++) glds(xo[j], (grp ? xb : xa) + ss * 128, lds0 + (s & 3) * 32768 + grp * 8192 + rw * 2048 + j * 1024);
      };
      auto issue_w = [&](int s) {
        const int ss = (s + rot1) & 7;
#pragma unroll
        for (int j = 0; j < 4; j++) glds(wo, wb + (size_t)(ss * 64 + 4 * j) * 3584, lds0 + (s & 3) * 32768 + 16384 + rw * 4096 + j * 1024);
      };
      TR(1);
      issue_x(0, 0); issue_w(0); issue_x(0, 1);
      START_BARRIER();
      auto step = [&](int s) {
        if (s >= 3 && !(prog_ok(cB, s - 2) && prog_ok(cA, s - 2))) {
          WAITV(0);
          set_prog(myprog, s);
          wait_prog(bprog, s - 2, cB);
          wait_prog(aprog, s - 2, cA);
        }
        TR(2 * s);
        issue_x(s, 0); issue_w(s);
        if (s < 4) issue_x(s, 1);
      };
      auto landed = [&](int s) {
        set_prog(myprog, s);
        TR(2 * s + 1);
      };
#define PEEK() const v2u pv = *(volatile LAS v2u*)aprog
#define TAKE() cA = __builtin_amdgcn_readfirstlane(pv.x); cB = __builtin_amdgcn_readfirstlane(pv.y)
#pragma unroll
      for (int s = 1; s < 8; s++) {
        step(s);
        PEEK();
        if (s == W2POS + 1) { if (s < 4) WAITV(16); else WAITV(14); }
        else if (s < 4) WAITV(8);
        else WAITV(6);
        landed(s);
        if (s == W2POS) { ldw2(0); ldw2(1); }
        TAKE();
      }
#pragma unroll
      for (int s = 4; s < 8; s++) issue_x(s, 1);
      WAITV(8); set_prog(myprog, 8);
      TR(16);
      ldw2(2);
      WAITV(8); set_prog(myprog, 9);
      TR(17);
      ldw2(3);
      WAITV(8); set_prog(myprog, 10);
      TR(18);
    } else {
      const int u = (xcd >> 1) * 4 + pl, unit = 14 * fh + umin(u, 13);
      const rsrc_t rw1 = __builtin_amdgcn_make_buffer_rsrc((void*)w1, 0, F_ * D_ * 2, 0x00020000);
      v4u t[16];
      START_BARRIER();
#pragma unroll
      for (int j = 0; j < 16; j++) t[j] = ldp128(rw1, (128 * rw + (lane >> 3)) * 3584 + unit * 128 + (lane & 7) * 16, j * 8 * 3584);
      float a[8];
      unsigned sel_lo = 0x00003f80u, sel_hi = 0x3f800000u;
      asm volatile("" : "+v"(sel_lo), "+v"(sel_hi));
#pragma unroll
      for (int e = 0; e < 8; e++) a[e] = 0.f;
#pragma unroll
      for (int j = 0; j < 16; j++)
#pragma unroll
        for (int e = 0; e < 4; e++) { a[2 * e] = dot2(t[j][e], sel_lo, a[2 * e]); a[2 * e + 1] = dot2(t[j][e], sel_hi, a[2 * e + 1]); }
      lds_st<f4>(lds0 + rw * 2048 + lane * 32, f4{a[0], a[1], a[2], a[3]});
      lds_st<f4>(lds0 + rw * 2048 + lane * 32 + 16, f4{a[4], a[5], a[6], a[7]});
      set_prog(myprog, 1);
      wait_prog(lprog, 1, cL);
      float sm = 0.f;
#pragma unroll
      for (int j = 0; j < 8; j++) sm += lds_ld<float>(lds0 + (8 * g + j) * 256 + (16 * rw + n16) * 4);
      sm = add_x32(add_x16(sm));
      set_prog(myprog, 2);
      const unsigned tag = get_tag();
      st64(rs, v2u{f2u(sm), tag}, (lane < 16 && u < 14) ? lane * 8 : OOB, WS_CSUM + (unit * 64 + 16 * rw) * 8);
      TR(2);
      wait_prog(lprog, 2, cL);
#pragma unroll
      for (int u2 = 0; u2 < 4; u2++) ldw2(u2);
    }
    {
      __builtin_amdgcn_s_setprio(PRIO_L2);
#pragma unroll
      for (int u = 4; u < 7; u++) ldw2(u);
      set_prog(myprog, 11);
      TR(19);
      const unsigned wdst = lds0 + rw * 4096 + (lane >> 3) * 128 + (((lane & 7) ^ (((lane >> 4) & 1) << 2)) << 4);
#pragma unroll
      for (int u = 0; u < 7; u++) {
        if (!(u & 1)) { wait_prog(bprog, 6 + (u >> 1), cB); wait_prog(aprog, 6 + (u >> 1), cA); TR(20 + (u >> 1)); }
#pragma unroll
        for (int j = 0; j < 4; j++) lds_st<v4u>(wdst + u * 16384 + j * 1024, wq[u][j]);
      }
      set_prog(myprog, 15);
    }
    ROLE_END();
  } else if (role == 0) {
    __builtin_amdgcn_s_setprio(PRIO_X);
    const unsigned myprog = xprog + rw, pmask = 0x80u << (8 * (rw ^ 1));
    const int cp = b, c = rw >> 1, mh = rw & 1, ch = 2 * cp + c;
    unsigned sdt, sD, ep;
    asm volatile("s_waitcnt lgkmcnt(0)\n\ts_load_dword %2, %6, 0x0\n\ts_load_dword %0, %3, %5\n\ts_load_dword %1, %4, %5\n\ts_barrier\n\ts_waitcnt lgkmcnt(0)"
                 : "=&s"(sdt), "=&s"(sD), "=&s"(ep) : "s"(logdt), "s"(Dsk), "s"(b * 4), "s"(ws) : "memory");
    if (rw == 0) lds_st<v2u>(lds0 + LD_TAG, v2u{ep + 1u, 1u});
    const unsigned pdt = c ? sdt >> 16 : sdt & 0xffffu, pD = c ? sD >> 16 : sD & 0xffffu;
    asm volatile("s_waitcnt vmcnt(1)" : "+v"(pA), "+v"(pI), "+v"(pCr), "+v"(pCi), "+v"(pf)::"memory");
    const unsigned scr = lds0 + LD_KT + c * KT_CH + mh * KT_COPY;
    const unsigned oscr = lds0 + LD_KT + c * KT_CH + (mh ^ 1) * KT_COPY;
    const int tok0 = 512 * rw + 8 * lane;
    v4u gq[4], xv[4];
    auto gather = [&]() {
#pragma unroll
      for (int j = 0; j < 4; j++) gq[j] = ld128(rs, WS_GRAN + lane * 16, (rw * 4 + j) * 1024);
#pragma unroll
      for (int j = 0; j < 4; j++) xv[j] = ld128(rs, (tok0 >> 5) * 128 + (lane & 3) * 16 + (j >> 1) * 64, WS_XT + cp * 16384 + (j & 1) * 8192);
      CFENCE();
    };
    bool early = false;
    f4 kd[3];
    {
      const float dt = rbf(__builtin_amdgcn_exp2f(bflo(pdt) * kLog2e));
      const float lr = -rbf(__builtin_amdgcn_exp2f(bflo(pA) * kLog2e)) * dt * kLog2e;
      const float rev = bflo(pI) * dt * 0.15915494309189535f;
      TR(1);
      cpx C = {bflo(pCr) * dt, bflo(pCi) * dt};
      const unsigned eb = umin(umax(wave_umax(umax(f2u(C.r) & 0x7fffffffu, f2u(C.i) & 0x7fffffffu)) >> 23, 12u), 254u);
      const float sdn = u2f((266u - eb) << 23), sup = u2f((eb - 11u) << 23);
      C.r *= sdn; C.i *= -sdn;
      const cpx e8 = cexp_at(lr, rev, 8.f);
      const cpx e1 = cexp_at(lr, -rev, 1.f), e2 = cmul(e1, e1), e4 = cmul(e2, e2), gg = cexp_at(lr, -rev, 128.f);
      const unsigned wa = scr + lane * 4, ra = scr + n16 * 272 + g * 16;
      auto put = [&](int i, cpx v) { lds_st<unsigned>(wa + i * 272, pkh(v.r, v.i)); };
      v4u fq[4];
      {
        const cpx e16 = cmul(e8, e8);
        cpx a = e16, bq = cmul(e8, e16);
        put(0, cpx{1.f, 0.f}); put(1, e8);
#pragma unroll
        for (int j = 2; j < 16; j += 2) {
          put(j, a); put(j + 1, bq);
          if (j < 14) { a = cmul(a, e16); bq = cmul(bq, e16); }
        }
        CFENCE();
#pragma unroll
        for (int s = 0; s < 4; s++) fq[s] = lds_ld<v4u>(ra + s * 64);
        CFENCE();
      }
      auto tile = [&](int T) {
        CFENCE();
        v4u fp[4];
#pragma unroll
        for (int s = 0; s < 4; s++) fp[s] = lds_ld<v4u>(ra + s * 64);
        CFENCE();
        f4 a = {0.f, 0.f, 0.f, 0.f};
#pragma unroll
        for (int s = 0; s < 4; s++) a = mfma16h(fp[s], fq[s], a);
        kd[T] = a * sup;
      };
      cpx P = C;
      {
        cpx a = P, bq = cmul(P, gg);
        P = cmul(bq, gg);
#pragma unroll
        for (int j = 0; j < 8; j++) {
          put(j, a); put(8 + j, bq);
          if (j < 7) { a = cmul(a, e1); bq = cmul(bq, e1); }
        }
      }
      tile(0);
      const cpx P2 = P, P3 = cmul(P, gg), P4 = cmul(P3, gg);
      P = P4;
#pragma unroll
      for (int j = 0; j < 12; j++) {
        put(4 * (j & 3) + (j < 4 ? 0 : 1 + (j >> 2)), P);
        if (j < 4) put(4 * j + 1, cmul(P, e4));
        P = cmul(P, gg);
      }
      {
        const unsigned pv = *(volatile LAS unsigned*)lprog;
        tile(1);
        cL = __builtin_amdgcn_readfirstlane(pv);
        early = prog_ok(cL, has1 ? XGATE : IGATE);
        asm volatile("s_waitcnt vmcnt(0)" : "+v"(pf)::"memory");
        if (early) { TR(9); gather(); }
      }
      {
        cpx a = P2, bq = P3;
#pragma unroll
        for (int j = 0; j < 4; j++) {
          put(j, a); put(4 + j, bq);
          if (j < 3) { a = cmul(a, e2); bq = cmul(bq, e2); }
        }
        put(8, P4); put(9, P);
      }
      tile(2);
    }
    TR(2);
    if (!early) {
      wait_prog(lprog, has1 ? XGATE : IGATE, cL);
      TR(10);
      gather();
      TR(11);
    }
    const unsigned ut = lds0 + LD_UT + c * UT_CH, kt = lds0 + LD_KT + c * KT_CH;
    if (mh == 0) {
      lds_st<f4>(scr + 1024 + lane * 16, kd[1]);
      lds_st<float>(scr + 2048 + lane * 4, kd[2][1]);
      lds_st<v4u>(ut + umin(lane, 29) * 16, v4u{0u, 0u, 0u, 0u});
      set_prog(myprog, 2);
      wait_prog(xprog, 2, cX, pmask);
      f4 v = kd[0] + lds_ld<f4>(oscr + 3072 + lane * 16);
      const f4 u = kd[2] + lds_ld<f4>(oscr + 4096 + lane * 16);
      CFENCE();
      v[0] += lane == 0 ? bflo(pD) : 0.f;
      const float nx = u2f(__builtin_amdgcn_update_dpp(0u, f2u(u[0]), 0x130, 0xf, 0xf, false));
      const unsigned a1 = lane < 32 ? kt + 3568 - 16 * lane : lds0 + LD_DUMP;
      const unsigned u22 = pkbf(u[2], u[2]), u00 = pkbf(u[0], u[0]);
      lds_st<v4u>(a1, v4u{pkbf(nx, u[3]), u22, pkbf(u[2], u[1]), u00});
      lds_st<v4u>(lane < 32 ? a1 + KT_COPY : a1, v4u{pkbf(u[3], u[2]), u22, pkbf(u[1], u[0]), u00});
      CFENCE();
      const unsigned a0 = kt + 4088 - 2 * (128 * (g >> 1) + 8 * n16 + 4 * (g & 1));
      const unsigned v32 = pkbf(v[3], v[2]), v10 = pkbf(v[1], v[0]);
      lds_st<v2u>(a0, v2u{v32, v10});
      lds_st<u16>(a0 + KT_COPY - 2, (u16)v32);
      lds_st<unsigned>(a0 + KT_COPY, pkbf(v[2], v[1]));
      lds_st<u16>(a0 + KT_COPY + 4, (u16)(v10 >> 16));
      lds_st<u16>(kt + KT_COPY + 4094, (u16)0);
      lds_st<unsigned>(kt + 4096 + (lane & 7) * 4 + ((lane >> 3) & 1) * KT_COPY, 0u);
    } else {
      lds_st<f4>(scr + 3072 + lane * 16, kd[0]);
      lds_st<f4>(scr + 4096 + lane * 16, kd[2]);
      set_prog(myprog, 2);
      wait_prog(xprog, 2, cX, pmask);
      const f4 v = kd[1] + lds_ld<f4>(oscr + 1024 + lane * 16);
      const float kend = u2f(__builtin_amdgcn_readlane(f2u(kd[2][1]), 32)) + lds_ld<float>(oscr + 2048 + 128);
      CFENCE();
      auto next = [&](float cur, unsigned last) { return u2f(__builtin_amdgcn_update_dpp(last, f2u(cur), 0x130, 0xf, 0xf, false)); };
      auto rol = [&](float t) { return __builtin_amdgcn_update_dpp(0u, f2u(t), 0x134, 0xf, 0xf, false); };
      const float n0 = next(v[0], rol(v[2])), n2 = next(v[2], rol(v[3])), n3 = next(v[3], f2u(kend));
      const unsigned a1 = kt + 1008 - 16 * lane;
      {
        const unsigned v11 = pkbf(v[1], v[1]), v00 = pkbf(v[0], v[0]);
        lds_st<v4u>(a1 + 2048, v4u{pkbf(n0, n0), v11, pkbf(v[1], v[0]), v00});
        lds_st<v4u>(a1 + 2048 + KT_COPY, v4u{pkbf(n0, v[1]), v11, v00, v00});
      }
#pragma unroll
      for (int r = 2; r < 4; r++) {
        const float nx = r == 2 ? n2 : n3, z = (lane & 1) ? nx : v[r];
        const unsigned nn = pkbf(nx, nx), vv = pkbf(v[r], v[r]);
        lds_st<v4u>(a1 + (3 - r) * 1024, v4u{nn, pkbf(nx, z), vv, vv});
        lds_st<v4u>(a1 + (3 - r) * 1024 + KT_COPY, v4u{nn, pkbf(z, v[r]), vv, vv});
      }
    }
    TR(3);
    const unsigned tag = get_tag();
    auto gathered = [&]() {
      unsigned bad = 0;
#pragma unroll
      for (int j = 0; j < 4; j++) bad |= (gq[j].y ^ tag) | (gq[j].w ^ tag) | (xv[j].y ^ tag) | (xv[j].w ^ tag);
      return wall(bad == 0);
    };
    if (!gathered()) {
#pragma unroll 1
      for (unsigned i = 0; i < kMemSpin; ++i) {
        __builtin_amdgcn_s_sleep(8);
        TR(12);
        gather();
        if (gathered()) break;
      }
    }
    TR(4);
    {
      float mr[8], ri[8];
#pragma unroll
      for (int j = 0; j < 4; j++) {
        ri[2 * j] = __builtin_amdgcn_rcpf(bfhi(gq[j].x)); mr[2 * j] = -bflo(gq[j].x) * ri[2 * j];
        ri[2 * j + 1] = __builtin_amdgcn_rcpf(bfhi(gq[j].z)); mr[2 * j + 1] = -bflo(gq[j].z) * ri[2 * j + 1];
      }
#pragma unroll
      for (int k = 0; k < 2; k++) {
        v4u o;
#pragma unroll
        for (int j = 0; j < 4; j++) {
          const unsigned d = xv[2 * k + (j >> 1)][2 * (j & 1)];
          o[j] = pkbf(bflo(d) * ri[2 * j] + mr[2 * j], bfhi(d) * ri[2 * j + 1] + mr[2 * j + 1]);
        }
        lds_st<v4u>(lds0 + LD_UT + k * UT_CH + 480 + tok0 * 2, o);
      }
      set_prog(myprog, 5);
      if ((tb >> 3) == rw) {
        const unsigned ar = lds0 + ((tok0 >> 6) == tb ? LD_RS : LD_DUMP) + (tok0 & 63) * 4;
        lds_st<f4>(ar, f4{ri[0], ri[1], ri[2], ri[3]});
        lds_st<f4>(ar + 16, f4{ri[4], ri[5], ri[6], ri[7]});
      }
      wait_prog(xprog, 5, cX);
      if (b == 0 && rw == 0) st32(rs, tag, lane == 0 ? 0 : OOB, WS_EPOCH);
    }
    TR(5);
    __builtin_amdgcn_s_setprio(PRIO_X2);
    f4 ya[8];
#pragma unroll
    for (int i = 0; i < 8; i++) ya[i] = f4{0.f, 0.f, 0.f, 0.f};
    {
      const unsigned ka = lds0 + LD_KT + c * KT_CH + (1 - (n16 & 1)) * KT_COPY + 4 * (1015 - (n16 >> 1) + 4 * g) - 64 * (59 + 4 * mh);
      const unsigned ua = ut + 2 * (224 + 16 * n16 + 8 * g) - 64 * (3 + 4 * mh);
      unsigned kb[4];
#pragma unroll
      for (int j = 0; j < 4; j++) { kb[j] = ka + 1024 * j; asm volatile("" : "+v"(kb[j])); }
      typedef unsigned v2a __attribute__((ext_vector_type(2), aligned(4)));
      v2u kl[2][8], kh_[2][8];
      s8 uf[2][8];
#define KL(r, k) kl[(r) & 1][k] = *(LAS v2a*)(kb[(7 - (k)) >> 1] + 64 * (3 - (r)) + 512 * ((7 - (k)) & 1))
#define KH(r, k) kh_[(r) & 1][k] = *(LAS v2a*)(kb[(7 - (k)) >> 1] + 64 * (3 - (r)) + 512 * ((7 - (k)) & 1) + 8)
#define UL(r, e) uf[(r) & 1][e] = lds_ld<s8>(ua + 64 * (3 - (r)) + 512 * (e))
#define MM(r, k, e) SCHED(); ya[(k) + (e)] = mfma16(__builtin_bit_cast(s8, v4u{kl[(r) & 1][k].x, kl[(r) & 1][k].y, kh_[(r) & 1][k].x, kh_[(r) & 1][k].y}), uf[(r) & 1][e], ya[(k) + (e)]); SCHED()
      KL(0, 0); KH(0, 0); UL(0, 7); UL(0, 6); UL(0, 5); UL(0, 4); UL(0, 3); UL(0, 2); UL(0, 1); UL(0, 0); KL(0, 1); KH(0, 1);
      MM(0, 0, 7); MM(0, 0, 6); MM(0, 0, 5); MM(0, 0, 4); MM(0, 0, 3); KH(0, 2); MM(0, 0, 2); KL(0, 2); MM(0, 0, 1); MM(0, 0, 0);
      MM(0, 1, 6); MM(0, 1, 5); MM(0, 1, 4); KH(0, 3); MM(0, 1, 3); KL(0, 3); MM(0, 1, 2); MM(0, 1, 1); MM(0, 1, 0);
      MM(0, 2, 5); KH(0, 4); MM(0, 2, 4); KL(0, 4); MM(0, 2, 3); KH(0, 5); MM(0, 2, 2); KL(0, 5); MM(0, 2, 1); KH(0, 6);
      MM(0, 2, 0); KL(0, 6); MM(0, 3, 4); KH(0, 7); MM(0, 3, 3); KL(0, 7); MM(0, 3, 2); KH(1, 0); MM(0, 3, 1); KL(1, 0);
      MM(0, 3, 0); UL(1, 7); MM(0, 4, 3); UL(1, 6); MM(0, 4, 2); KH(1, 1); MM(0, 4, 1); KL(1, 1); MM(0, 4, 0); UL(1, 5);
      MM(0, 5, 2); KH(1, 2); MM(0, 5, 1); KL(1, 2); MM(0, 5, 0); UL(1, 4); MM(0, 6, 1); MM(0, 6, 0); KH(1, 3); MM(0, 7, 0); KL(1, 3);
      MM(1, 0, 7); UL(1, 3); MM(1, 0, 6); MM(1, 1, 6); MM(1, 0, 5); KH(1, 4); MM(1, 1, 5); KL(1, 4); MM(1, 2, 5); UL(1, 2); MM(1, 0, 4);
      MM(1, 1, 4); MM(1, 2, 4); MM(1, 3, 4); KH(1, 5); MM(1, 0, 3); KL(1, 5); MM(1, 1, 3); UL(1, 1); MM(1, 2, 3); MM(1, 3, 3);
      MM(1, 4, 3); MM(1, 0, 2); MM(1, 1, 2); KH(1, 6); MM(1, 2, 2); KL(1, 6); MM(1, 3, 2); UL(1, 0); MM(1, 4, 2); MM(1, 5, 2);
      MM(1, 0, 1); KH(1, 7); MM(1, 1, 1); KL(1, 7); MM(1, 2, 1); KH(2, 0); MM(1, 3, 1); KL(2, 0); MM(1, 4, 1); UL(2, 7);
      MM(1, 5, 1); UL(2, 6); MM(1, 6, 1); UL(2, 5); MM(1, 0, 0); UL(2, 4); MM(1, 1, 0); UL(2, 3); MM(1, 2, 0); UL(2, 2);
      MM(1, 3, 0); UL(2, 1); MM(1, 4, 0); UL(2, 0); MM(1, 5, 0); KH(2, 1); MM(1, 6, 0); KL(2, 1); MM(1, 7, 0); MM(2, 0, 7); MM(2, 0, 6);
      MM(2, 0, 5); MM(2, 0, 4); MM(2, 0, 3); KH(2, 2); MM(2, 0, 2); KL(2, 2); MM(2, 0, 1); MM(2, 0, 0); MM(2, 1, 6); MM(2, 1, 5);
      MM(2, 1, 4); KH(2, 3); MM(2, 1, 3); KL(2, 3); MM(2, 1, 2); MM(2, 1, 1); MM(2, 1, 0); MM(2, 2, 5); KH(2, 4); MM(2, 2, 4); KL(2, 4);
      MM(2, 2, 3); KH(2, 5); MM(2, 2, 2); KL(2, 5); MM(2, 2, 1); KH(2, 6); MM(2, 2, 0); KL(2, 6); MM(2, 3, 4); KH(2, 7);
      MM(2, 3, 3); KL(2, 7); MM(2, 3, 2); KH(3, 0); MM(2, 3, 1); KL(3, 0); MM(2, 3, 0); UL(3, 7); MM(2, 4, 3); UL(3, 6);
      MM(2, 4, 2); KH(3, 1); MM(2, 4, 1); KL(3, 1); MM(2, 4, 0); UL(3, 5); MM(2, 5, 2); KH(3, 2); MM(2, 5, 1); KL(3, 2);
      MM(2, 5, 0); UL(3, 4); MM(2, 6, 1); MM(2, 6, 0); KH(3, 3); MM(2, 7, 0); KL(3, 3); MM(3, 0, 7); UL(3, 3); MM(3, 0, 6); MM(3, 1, 6);
      MM(3, 0, 5); KH(3, 4); MM(3, 1, 5); KL(3, 4); MM(3, 2, 5); UL(3, 2); MM(3, 0, 4); MM(3, 1, 4); MM(3, 2, 4); MM(3, 3, 4); KH(3, 5);
      MM(3, 0, 3); KL(3, 5); MM(3, 1, 3); UL(3, 1); MM(3, 2, 3); MM(3, 3, 3); MM(3, 4, 3); MM(3, 0, 2); MM(3, 1, 2); KH(3, 6);
      MM(3, 2, 2); KL(3, 6); MM(3, 3, 2); UL(3, 0); MM(3, 4, 2); MM(3, 5, 2); MM(3, 0, 1); MM(3, 1, 1); MM(3, 2, 1);
      MM(3, 3, 1); KH(3, 7); MM(3, 4, 1); KL(3, 7); set_prog(myprog, 6); MM(3, 5, 1); MM(3, 6, 1); MM(3, 0, 0); MM(3, 1, 0);
      MM(3, 2, 0); MM(3, 3, 0); MM(3, 4, 0); MM(3, 5, 0); MM(3, 6, 0); MM(3, 7, 0);
#undef KL
#undef KH
#undef UL
#undef MM
      SCHED();
    }
    __builtin_amdgcn_s_setprio(3);
    TR(6);
    wait_prog(xprog, 6, cX);
    {
      f4 yk[4];
      if (mh == 0) {
#pragma unroll
        for (int j = 0; j < 4; j++) { lds_st<f4>(scr + j * 1024 + lane * 16, ya[4 + j]); yk[j] = ya[j]; }
      } else {
#pragma unroll
        for (int j = 0; j < 4; j++) { lds_st<f4>(scr + j * 1024 + lane * 16, ya[j]); yk[j] = ya[4 + j]; }
      }
      set_prog(myprog, 7);
      wait_prog(xprog, 7, cX, pmask);
#pragma unroll
      for (int j = 0; j < 4; j++) yk[j] += lds_ld<f4>(oscr + j * 1024 + lane * 16);
      set_prog(myprog, 8);
#pragma unroll
      for (int j = 0; j < 4; j++)
        st128(rs, v4u{pkbf(yk[j][0], yk[j][1]), tag, pkbf(yk[j][2], yk[j][3]), tag}, (4 * n16 + g) * 16 + j * 1024, WS_YT + ch * 8192 + mh * 4096);
    }
    TR(7);
    wait_prog(bprog, YGATE, cB);
    TR(14);
    {
      v4u y[4];
#pragma unroll 1
      for (unsigned i = 0; i < kMemSpin; ++i) {
        CFENCE();
#pragma unroll
        for (int j = 0; j < 4; j++) y[j] = ld128(rs, g * 8192 + n16 * 16, WS_YT + (64 * cb + 16 * rw + 4 * j) * 8192 + tb * 256);
        CFENCE();
        unsigned bad = 0;
#pragma unroll
        for (int j = 0; j < 4; j++) bad |= (y[j].y ^ tag) | (y[j].w ^ tag);
        if (wall(bad == 0)) break;
        wait_prog(bprog, 13, cB);
        __builtin_amdgcn_s_sleep(2);
      }
      TR(15);
      const unsigned ya0 = lds0 + LD_UT + (16 * rw + g) * 128 + (((n16 >> 3) ^ (g >> 1)) << 6) + (n16 & 7) * 8;
#pragma unroll
      for (int j = 0; j < 4; j++) lds_st<v2u>(ya0 + j * 512, v2u{y[j].x, y[j].z});
      set_prog(myprog, 9);
    }
    ROLE_END();
  } else {
    const bool isA = role == 2;
    __builtin_amdgcn_s_setprio(3);
    const unsigned gprog = isA ? aprog : bprog, myprog = gprog + rw, pmask = 0x80u << (8 * (rw ^ 1));
    const int th = rw >> 1, kh = rw & 1;
    const int tbk = isA ? tb ^ 1 : tb;
    unsigned cG = 0x80808080u;
    v2u xres[4];
#pragma unroll
    for (int j = 0; j < 4; j++) xres[j] = v2u{0u, 0u};
    START_BARRIER();
    const unsigned tag = get_tag();
    asm volatile("s_waitcnt vmcnt(0)" : "+v"(i0), "+v"(i1)::"memory");
    TR(1);
    if (isA) {
      float s1 = 0.f, s2 = 0.f;
#pragma unroll
      for (int j = 0; j < 4; j++) {
        s1 = dot2(i1[j], 0x3f803f80u, dot2(i0[j], 0x3f803f80u, s1));
        s2 = dot2(i1[j], i1[j], dot2(i0[j], i0[j], s2));
      }
      const float m = add_x16(sum16(s1)) * (1.f / D_);
      const float var = rbf(__builtin_fmaxf(add_x16(sum16(s2)) * (1.f / D_) - m * m, 0.f));
      const unsigned gv = pkbf(m, __builtin_amdgcn_sqrtf(rbf(var + 1e-5f)));
      const int sbk = 8 * tb + cb;
      st64(rs, v2u{gv, tag}, n32 == 0 ? hh * 8 : OOB, WS_GRAN + (((sbk >> 6) * 4 + rw) * 64 + (sbk & 63)) * 16);
    } else {
      const unsigned scr = lds0 + 7 * 16384 + rw * 2048;
      lds_st<v4u>(scr + lane * 16, i0);
      lds_st<v4u>(scr + 1024 + lane * 16, i1);
      CFENCE();
#pragma unroll
      for (int j = 0; j < 2; j++) {
        const int cho = 64 * cb + 32 * kh + 16 * j + n16;
#pragma unroll
        for (int hf2 = 0; hf2 < 2; hf2++) {
          const v2u tr = __builtin_bit_cast(v2u, tr_ld(scr + (8 * g + q) * 64 + 32 * j + 8 * pp + 256 * hf2));
          st128(rs, v4u{tr.x, tag, tr.y, tag}, (cho >> 1) * 16384 + (cho & 1) * 64 + g * 16, WS_XT + hf2 * 8192 + (2 * tb + th) * 128);
        }
      }
#pragma unroll
      for (int j = 0; j < 4; j++) xres[j] = lds_ld<v2u>(scr + n32 * 64 + 16 * j + 8 * hh);
    }
    set_prog(myprog, has1 ? 1 : 9);
    __builtin_amdgcn_s_setprio(PRIO_A);
    TR(2);
    constexpr int WIN = 3;
    v4u hf[WIN][4];
    const int hb = WS_H + ((2 * tbk + th) * 112 + 56 * fh + 4 * kh) * 1024;
    const int hfl = WS_HFLAG + ((tbk * 2 + fh) * 4 + rw) * 1024;
    if (has1) {
      f16v acc[2];
#pragma unroll
      for (int i = 0; i < 16; i++) acc[0][i] = acc[1][i] = 0.f;
      const Geo G = {lds0, lane, rw, hh, blk, q, pp, g, n16, n32};
      const int t = 64 * tbk + 32 * th + n32;
      const int mo = WS_GRAN + ((((t >> 9) * 4 + ((t >> 1) & 3)) * 64 + ((t >> 3) & 63)) * 2 + (t & 1)) * 8;
      const int co = WS_CSUM + (slice * 128 + 64 * kh + lane) * 8;
      v2u gm, gc;
      auto correct = [&]() {
        if (__builtin_expect(!wall(gm.y == tag && gc.y == tag), 0)) {
#pragma unroll 1
          for (unsigned i = 0; i < kMemSpin; ++i) {
            __builtin_amdgcn_s_sleep(4);
            CFENCE();
            gm = ld64(rs, mo, 0); gc = ld64(rs, co, 0);
            if (wall(gm.y == tag && gc.y == tag)) break;
          }
        }
        const unsigned m = (gm.x ^ 0x8000u) & 0xffffu;
        const s8 cm = __builtin_bit_cast(s8, v4u{m | (m << 16), m, 0u, 0u});
        const float cs = u2f(gc.x), c0 = rbf(cs), c1 = rbf(cs - c0);
        const unsigned p0 = pkbf(c0, c1), p1 = pkbf(cs - c0 - c1, 0.f);
        acc[0] = mfma32(__builtin_bit_cast(s8, v4u{hh ? 0u : p0, hh ? 0u : p1, 0u, 0u}), cm, acc[0]);
        acc[1] = mfma32(__builtin_bit_cast(s8, v4u{hh ? p0 : 0u, hh ? p1 : 0u, 0u, 0u}), cm, acc[1]);
      };
      if (isA) {
        gemm1_loop<true>(G, acc, cL, [&](int s) {
          if (s >= 8) TR(s == 16 ? 3 : s - 4);
          if (s == 4) { gm = ld64(rs, mo, 0); gc = ld64(rs, co, 0); }
          if (s == 6) correct();
        });
      } else {
        gemm1_loop<false>(G, acc, cL, [&](int s) {
          if (s >= 8) TR(s == 16 ? 3 : s - 4);
          if (s == 3) { wait_prog(lprog, 5, cL); gm = ld64(rs, mo, 0); gc = ld64(rs, co, 0); correct(); __builtin_amdgcn_s_setprio(PRIO_B); }
        });
      }
      TR(12);
#pragma unroll
      for (int s = 0; s < 4; s++) {
#pragma unroll
        for (int j = 0; j < 4; j++) {
          const unsigned p = pkbf(acc[s >> 1][8 * (s & 1) + 2 * j], acc[s >> 1][8 * (s & 1) + 2 * j + 1]);
          hf[0][s][j] = __builtin_bit_cast(unsigned, __builtin_elementwise_max(__builtin_bit_cast(sh2, p), sh2{0, 0}));
        }
        st128(rs, hf[0][s], lane * 16 + s * 1024, hb + cb * 8192);
      } TR(13);
      WAITV(0);
      TR(14);
      st32(rs, tag, lane == 0 ? 0 : OOB, hfl + cb * 128);
    }
    wait_prog(lprog, 11, cL);
    if (!isA) { __builtin_amdgcn_s_setprio(PRIO_B); wait_prog(aprog, BGATE, cA); set_prog(myprog, 11); }
    TR(15);
    poll_tag(rs, hfl + umin(lane, 6) * 128, tag);
    TR(16);
    TR(17);
    auto ldh = [&](int c, int s) {
      int sg = c + cb; sg = sg >= 7 ? sg - 7 : sg;
      hf[c % WIN][s] = ld128(rs, lane * 16 + s * 1024, hb + sg * 8192);
    };
    if (!has1) {
#pragma unroll
      for (int s = 0; s < 4; s++) ldh(0, s);
    }
#pragma unroll
    for (int c = 1; c < WIN; c++)
#pragma unroll
      for (int s = 0; s < 4; s++) { ldh(c, s); SCHED(); }
    set_prog(myprog, 12);
    unsigned a2[2];
    int un = cb + 7 - 2 * pl; un = un >= 7 ? un - 7 : un; un = un >= 7 ? un - 7 : un;
#pragma unroll
    for (int ct = 0; ct < 2; ct++) a2[ct] = lds0 + un * 16384 + (64 * kh + 4 * hh + q) * 128 + ((ct ^ kh ^ 1 ^ (q >> 1)) << 6) + 32 * blk + 8 * pp;
    constexpr int PD2 = 2;
    s8 fw2[PD2 + 1][2];
    auto rd2 = [&](int i) {
      if (i > 0 && (i & 3) == 0) {
        const int d = un == 6 ? -6 * 16384 : 16384;
        un = un == 6 ? 0 : un + 1;
        a2[0] += d; a2[1] += d;
      }
#pragma unroll
      for (int ct = 0; ct < 2; ct++) {
        const unsigned a = a2[ct] + (i & 3) * 2048;
        fw2[i % (PD2 + 1)][ct] = cat(tr_ld(a), tr_ld(a + 1024));
      }
    };
    wait_prog(lprog, 15, cL);
#pragma unroll
    for (int i = 0; i < PD2; i++) rd2(i);
    f16v ac2[2];
#pragma unroll
    for (int i = 0; i < 16; i++) ac2[0][i] = ac2[1][i] = 0.f;
    v4u pr[4];
    const int pro = isA ? OOB : lane * 16, prs = WS_PART + ((b ^ 1) * 4 + rw) * 4096;
    auto mm2 = [&](int i, int t) { ac2[t] = mfma32(fw2[i % (PD2 + 1)][t], __builtin_bit_cast(s8, hf[(i >> 2) % WIN][i & 3]), ac2[t]); };
#pragma unroll
    for (int i = 0; i < 25; i++) {
      const int c = i >> 2, s = i & 3;
      SCHED();
      mm2(i, 0); mm2(i, 1);
      SCHED();
      rd2(i + PD2);
      if (c + WIN < 7) ldh(c + WIN, s);
      if (c == 5) pr[s] = ld128(rs, pro + s * 1024, prs);
      if (s == 3) TR(18 + c);
      if (i == 15) set_prog(myprog, 13);
    }
    SCHED();
    rd2(27);
    if (!isA) wait_prog(xprog, 8, cX);
    SCHED();
    mm2(25, 0); mm2(26, 0); mm2(27, 0);
    SCHED();
    mm2(25, 1); mm2(26, 1); mm2(27, 1);
    SCHED();
    TR(24);
    const unsigned ex = (isA ? lds0 + 7 * 16384 : lds0 + LD_KT) + lane * 16;
#pragma unroll
    for (int j = 0; j < 4; j++) lds_st<f4>(ex + rw * 4096 + j * 1024, f4{ac2[0][4 * j], ac2[0][4 * j + 1], ac2[0][4 * j + 2], ac2[0][4 * j + 3]});
    set_prog(myprog, 14);
    TR(25);
    f16v& keep = ac2[1];
    if (isA) {
      wait_prog(gprog, 14, cG, pmask);
#pragma unroll
      for (int j = 0; j < 4; j++) {
        const f4 o = lds_ld<f4>(ex + (rw ^ 1) * 4096 + j * 1024);
        pr[j] = v4u{pkbf(keep[4 * j] + o[0], keep[4 * j + 1] + o[1]), tag, pkbf(keep[4 * j + 2] + o[2], keep[4 * j + 3] + o[3]), tag};
      }
#pragma unroll
      for (int j = 0; j < 4; j++) st128(rs, pr[j], lane * 16 + j * 1024, WS_PART + (b * 4 + rw) * 4096);
      TR(26);
      ROLE_END();
    } else {
      auto have = [&]() {
        unsigned bad = 0;
#pragma unroll
        for (int j = 0; j < 4; j++) bad |= (pr[j].y ^ tag) | (pr[j].w ^ tag);
        return wall(bad == 0);
      };
      if (!have()) {
#pragma unroll 1
        for (unsigned i = 0; i < kMemSpin; ++i) {
          CFENCE();
#pragma unroll
          for (int j = 0; j < 4; j++) pr[j] = ld128(rs, pro + j * 1024, prs);
          CFENCE();
          if (have()) break;
          __builtin_amdgcn_s_sleep(1);
        }
      }
      TR(26);
      wait_prog(xprog, 9, cX);
      TR(27);
      const float rstd = lds_ld<float>(lds0 + LD_RS + (32 * th + n32) * 4);
      const unsigned ay = lds0 + LD_UT + (32 * kh + 4 * hh + q) * 128 + ((th ^ (q >> 1)) << 6) + 32 * blk + 8 * pp;
      float base[16];
#pragma unroll
      for (int j = 0; j < 4; j++) {
        const v2u y = __builtin_bit_cast(v2u, tr_ld(ay + j * 1024));
        base[4 * j] = bflo(y.x) + bflo(xres[j].x); base[4 * j + 1] = bfhi(y.x) + bfhi(xres[j].x);
        base[4 * j + 2] = bflo(y.y) + bflo(xres[j].y); base[4 * j + 3] = bfhi(y.y) + bfhi(xres[j].y);
        keep[4 * j] += bflo(pr[j].x); keep[4 * j + 1] += bfhi(pr[j].x); keep[4 * j + 2] += bflo(pr[j].z); keep[4 * j + 3] += bfhi(pr[j].z);
      }
      wait_prog(gprog, 14, cG, pmask);
      TR(28);
      v2u pk[4];
#pragma unroll
      for (int j = 0; j < 4; j++) {
        const f4 o = lds_ld<f4>(ex + (rw ^ 1) * 4096 + j * 1024);
        pk[j].x = pkbf(base[4 * j] + rstd * (keep[4 * j] + o[0]), base[4 * j + 1] + rstd * (keep[4 * j + 1] + o[1]));
        pk[j].y = pkbf(base[4 * j + 2] + rstd * (keep[4 * j + 2] + o[2]), base[4 * j + 3] + rstd * (keep[4 * j + 3] + o[3]));
      }
      u16* op = out + (size_t)(64 * tb + 32 * th + n32) * D_ + 64 * cb + 32 * kh + 8 * hh;
#pragma unroll
      for (int j = 0; j < 4; j += 2) {
        const auto r0 = __builtin_amdgcn_permlane32_swap(pk[j].x, pk[j + 1].x, false, false);
        const auto r1 = __builtin_amdgcn_permlane32_swap(pk[j].y, pk[j + 1].y, false, false);
        const unsigned e0 = r0[0], e1 = r1[0], e2 = r0[1], e3 = r1[1];
        *(v4u*)(op + 8 * j) = v4u{e0, e1, e2, e3};
      }
      TR(29);
      ROLE_END();
    }
  }
}

}
#if defined(__HIP_DEVICE_COMPILE__)
#pragma clang attribute pop
#endif

extern "C" void kernel_launch(void* const* d_in, const int* in_sizes, int n_in, void* d_out, int out_size, void* d_ws,
                              size_t ws_size, hipStream_t stream) {
  (void)in_sizes; (void)n_in; (void)out_size; (void)ws_size;
  (void)hipFuncSetAttribute((const void*)fused_kernel, hipFuncAttributeMaxDynamicSharedMemorySize, LDS_BYTES);
  fused_kernel<<<256, 1024, LDS_BYTES, stream>>>((const u16*)d_in[0], (const u16*)d_in[1], (const u16*)d_in[2], (const u16*)d_in[3],
                                                (const u16*)d_in[4], (const u16*)d_in[5], (const u16*)d_in[6], (const u16*)d_in[8],
                                                (const u16*)d_in[9], (u16*)d_out, (char*)d_ws);
}
```

```cpp
#include <hip/hip_runtime.h>
#include <stdint.h>

#if defined(__HIP_DEVICE_COMPILE__)
#pragma clang attribute push(__attribute__((target("no-packed-fp32-ops"))), apply_to = function)
#endif
namespace {

typedef unsigned short u16;
typedef unsigned long long u64;
typedef short s4 __attribute__((ext_vector_type(4)));
typedef short s8 __attribute__((ext_vector_type(8)));
typedef short sh2 __attribute__((ext_vector_type(2)));
typedef __bf16 b2 __attribute__((ext_vector_type(2)));
typedef _Float16 h2 __attribute__((ext_vector_type(2)));
typedef _Float16 h8 __attribute__((ext_vector_type(8)));
typedef float f2 __attribute__((ext_vector_type(2)));
typedef float f4 __attribute__((ext_vector_type(4)));
typedef float f16v __attribute__((ext_vector_type(16)));
typedef unsigned v2u __attribute__((ext_vector_type(2)));
typedef unsigned v4u __attribute__((ext_vector_type(4)));
#define LAS __attribute__((address_space(3)))
#define DI __device__ __forceinline__

constexpr int D_ = 512, F_ = 1792, N_ = 128;
constexpr int kCodeLines = 210;

constexpr int WS_EPOCH = 0;
constexpr int WS_GRAN = 16384;
constexpr int WS_CSUM = 32768;
constexpr int WS_HFLAG = 65536;
constexpr int WS_XFLAG = WS_HFLAG + 2048 * 128;
constexpr int WS_YFLAG = WS_XFLAG + 1024 * 128;
constexpr int WS_PFLAG = WS_YFLAG + 1024 * 128;
constexpr int WS_XT = WS_PFLAG + 1024 * 128;
constexpr int WS_YT = WS_XT + 2 * 1024 * 1024;
constexpr int WS_PART = WS_YT + 2 * 1024 * 1024;
constexpr int WS_H = WS_PART + 2 * 1024 * 1024;
constexpr int WS_END = WS_H + 64 * 112 * 1024;

constexpr int LD_KT = 131072;
constexpr int KT_COPY = 5120 + 64;
constexpr int KT_CH = 2 * KT_COPY;
constexpr int LD_UT = LD_KT + 2 * KT_CH;
constexpr int UT_CH = 4576;
constexpr int LD_CTL = LD_UT + 2 * UT_CH;
constexpr int LD_APROG = LD_CTL, LD_BPROG = LD_CTL + 4, LD_LPROG = LD_CTL + 8, LD_XPROG = LD_CTL + 12, LD_TAG = LD_CTL + 16;
constexpr int LD_RS = LD_CTL + 64;
constexpr int LD_DUMP = LD_RS + 256;
constexpr int LD_END = LD_DUMP + 1024;
constexpr int LDS_BYTES = 163840;
static_assert(LD_END <= LDS_BYTES, "lds");
static_assert(LD_UT % 16 == 0 && LD_CTL % 16 == 0 && KT_COPY % 16 == 0, "align");

constexpr unsigned kLdsSpin = 1u << 20, kMemSpin = 1u << 16;

DI float u2f(unsigned v) { return __builtin_bit_cast(float, v); }
DI unsigned f2u(float v) { return __builtin_bit_cast(unsigned, v); }
DI float bflo(unsigned v) { return u2f(v << 16); }
DI float bfhi(unsigned v) { return u2f(v & 0xffff0000u); }
DI unsigned pkbf(float a, float b) { f2 v = {a, b}; return __builtin_bit_cast(unsigned, __builtin_convertvector(v, b2)); }
DI unsigned pkh(float a, float b) { f2 v = {a, b}; return __builtin_bit_cast(unsigned, __builtin_convertvector(v, h2)); }
DI float rbf(float a) { return bflo(pkbf(a, a)); }
DI float dot2(unsigned a, unsigned b, float acc) {
  return __builtin_amdgcn_fdot2_f32_bf16(__builtin_bit_cast(b2, a), __builtin_bit_cast(b2, b), acc, false);
}
DI unsigned umin(unsigned a, unsigned b) { return a < b ? a : b; }
DI unsigned umax(unsigned a, unsigned b) { return a > b ? a : b; }
DI bool wall(bool p) { return __builtin_amdgcn_ballot_w64(!p) == 0; }
template <class T> DI T lds_ld(unsigned a) { return *(LAS T*)(a); }
template <class T> DI void lds_st(unsigned a, T v) { *(LAS T*)(a) = v; }
DI s4 tr_ld(unsigned a) { return __builtin_amdgcn_ds_read_tr16_b64_v4i16((LAS s4*)(a)); }
DI s8 cat(s4 a, s4 b) { return __builtin_shufflevector(a, b, 0, 1, 2, 3, 4, 5, 6, 7); }
DI f16v mfma32(s8 a, s8 b, f16v c) { return __builtin_amdgcn_mfma_f32_32x32x16_bf16(a, b, c, 0, 0, 0); }
DI f4 mfma16(s8 a, s8 b, f4 c) { return __builtin_amdgcn_mfma_f32_16x16x32_bf16(a, b, c, 0, 0, 0); }
DI f4 mfma16h(v4u a, v4u b, f4 c) {
  return __builtin_amdgcn_mfma_f32_16x16x32_f16(__builtin_bit_cast(h8, a), __builtin_bit_cast(h8, b), c, 0, 0, 0);
}
#define SCHED() __builtin_amdgcn_sched_barrier(0)
#define PIN8(a, o) asm volatile("" : "+v"(a[o]), "+v"(a[o + 1]), "+v"(a[o + 2]), "+v"(a[o + 3]), "+v"(a[o + 4]), "+v"(a[o + 5]), "+v"(a[o + 6]), "+v"(a[o + 7]))
#define PIN16(a) do { PIN8(a, 0); PIN8(a, 8); } while (0)
#define CFENCE() asm volatile("" ::: "memory")
#define WAITV(n) asm volatile("s_waitcnt vmcnt(" #n ")" ::: "memory")

DI void set_prog(unsigned addr, unsigned v) { asm volatile("ds_write_b8 %0, %1" ::"v"(addr), "v"(v | 0x80u) : "memory"); }
DI void wait_prog(unsigned addr, unsigned need, unsigned& cache, unsigned mask = 0x80808080u) {
  unsigned t, cnt;
  cache = __builtin_amdgcn_readfirstlane(cache);
  asm volatile(
      "s_mov_b32 %[cnt], %[lim]\n"
      "1:\n\t"
      "s_sub_u32 %[t], %[c], %[n]\n\t"
      "s_andn2_b32 %[t], %[m], %[t]\n\t"
      "s_cbranch_scc0 2f\n\t"
      "ds_read_b32 v127, %[a]\n\t"
      "s_waitcnt lgkmcnt(0)\n\t"
      "v_readfirstlane_b32 %[c], v127\n\t"
      "s_sub_u32 %[cnt], %[cnt], 1\n\t"
      "s_cbranch_scc0 1b\n"
      "2:"
      : [c] "+&s"(cache), [t] "=&s"(t), [cnt] "=&s"(cnt)
      : [a] "v"(addr), [n] "s"(need * 0x01010101u), [m] "s"(mask), [lim] "i"(kLdsSpin)
      : "memory", "scc", "v127");
}
DI bool prog_ok(unsigned cache, unsigned need) { return (0x80808080u & ~(cache - need * 0x01010101u)) == 0; }
DI void glds(unsigned voff, const char* sbase, unsigned m0v) {
  asm volatile("s_mov_b32 m0, %2\n\ts_nop 0\n\tglobal_load_lds_dwordx4 %0, %1" ::"v"(voff), "s"(sbase), "s"(m0v) : "memory");
}
constexpr int SC1 = 16;
constexpr int OOB = 0x7ffffff0;
typedef __amdgpu_buffer_rsrc_t rsrc_t;
DI v4u ld128(rsrc_t r, int voff, int soff) { return __builtin_amdgcn_raw_buffer_load_b128(r, voff, soff, SC1); }
DI v2u ld64(rsrc_t r, int voff, int soff) { return __builtin_amdgcn_raw_buffer_load_b64(r, voff, soff, SC1); }
DI unsigned ld32(rsrc_t r, int voff, int soff) { return __builtin_amdgcn_raw_buffer_load_b32(r, voff, soff, SC1); }
DI void st128(rsrc_t r, v4u v, int voff, int soff) {
  __builtin_amdgcn_raw_buffer_store_b128(v, r, voff, soff, SC1);
  asm volatile("s_nop 1" ::"v"(v) : "memory");
}
DI void st64(rsrc_t r, v2u v, int voff, int soff) { __builtin_amdgcn_raw_buffer_store_b64(v, r, voff, soff, SC1); }
DI void st32(rsrc_t r, unsigned v, int voff, int soff) { __builtin_amdgcn_raw_buffer_store_b32(v, r, voff, soff, SC1); }
DI void st_flag(rsrc_t r, unsigned tag, int lane, int n, int soff) {
  v4u v = {tag, tag, tag, tag};
  asm volatile("" : "+v"(v));
  int o = lane < n ? lane * 16 : OOB;
  asm volatile("s_waitcnt vmcnt(0)" : "+v"(o)::"memory");
  st128(r, v, o, soff);
}
DI void st_preflag(rsrc_t r, unsigned tag, int voff, int soff) {
  v4u v = {~tag, ~tag, ~tag, ~tag};
  asm volatile("" : "+v"(v));
  st128(r, v, voff, soff);
}
DI v4u ldp128(rsrc_t r, int voff, int soff) { return __builtin_amdgcn_raw_buffer_load_b128(r, voff, soff, 0); }
template <int SLEEP> DI void wait_tag(rsrc_t r, int voff, unsigned tag) {
  CFENCE();
  if (__builtin_expect(!wall(ld32(r, voff, 0) == tag), 0)) {
#pragma unroll 1
    for (unsigned i = 0; i < kMemSpin; ++i) {
      __builtin_amdgcn_s_sleep(SLEEP);
      CFENCE();
      if (wall(ld32(r, voff, 0) == tag)) break;
    }
  }
  CFENCE();
}
template <int CTRL> DI float dpp(float v) { return u2f(__builtin_amdgcn_update_dpp(0u, f2u(v), CTRL, 0xf, 0xf, true)); }
DI float sum16(float v) { v += dpp<0xB1>(v); v += dpp<0x4E>(v); v += dpp<0x141>(v); return v + dpp<0x140>(v); }
DI float add_x16(float v) { const auto r = __builtin_amdgcn_permlane16_swap(f2u(v), f2u(v), false, false); const unsigned a = r[0], b = r[1]; return u2f(a) + u2f(b); }
DI float add_x32(float v) { const auto r = __builtin_amdgcn_permlane32_swap(f2u(v), f2u(v), false, false); const unsigned a = r[0], b = r[1]; return u2f(a) + u2f(b); }
template <int CTRL> DI unsigned dppmax(unsigned v) { return umax(v, __builtin_amdgcn_update_dpp(0u, v, CTRL, 0xf, 0xf, true)); }
DI unsigned wave_umax(unsigned v) {
  v = dppmax<0x140>(dppmax<0x141>(dppmax<0x4E>(dppmax<0xB1>(v))));
  return umax(umax(__builtin_amdgcn_readlane(v, 0), __builtin_amdgcn_readlane(v, 16)), umax(__builtin_amdgcn_readlane(v, 32), __builtin_amdgcn_readlane(v, 48)));
}
struct cpx { float r, i; };
DI cpx cmul(cpx a, cpx b) { return {a.r * b.r - a.i * b.i, a.r * b.i + a.i * b.r}; }
DI cpx cexp_at(float lr, float rev, float p) {
  const float e = __builtin_amdgcn_exp2f(lr * p);
  const float f = __builtin_amdgcn_fractf(rev * p);
  return {e * __builtin_amdgcn_cosf(f), e * __builtin_amdgcn_sinf(f)};
}
constexpr float kLog2e = 1.44269504088896341f;

DI int opaque(int v) { asm volatile("" : "+s"(v)); return v; }
DI int fresh_lane() { int l; asm volatile("v_mbcnt_lo_u32_b32 %0, -1, 0\n\tv_mbcnt_hi_u32_b32 %0, -1, %0" : "=v"(l)); return l & 63; }
struct Geo {
  unsigned lds0; int lane, rw, hh, blk, q, pp, g, n16, n32;
};

template <class F>
DI void gemm1_loop(const Geo& G, f16v (&acc)[2][2], unsigned& cL, F&& hook) {
  const int th = G.rw >> 1, kh = G.rw & 1;
  const unsigned lprog = G.lds0 + LD_LPROG, myprog = G.lds0 + LD_APROG + G.rw;
  unsigned ax[4], aw[2];
#pragma unroll
  for (int s = 0; s < 4; s++) ax[s] = G.lds0 + (32 * th + G.n32) * 128 + (((2 * s + G.hh) ^ ((G.n32 >> 1) & 7)) << 4);
#pragma unroll
  for (int ft = 0; ft < 2; ft++) aw[ft] = G.lds0 + 16384 + (8 * G.hh + G.q) * 256 + (((2 * kh + ft) ^ G.q) << 6) + 32 * G.blk + 8 * G.pp;
  s8 fx[2][2], fw[2][2];
  auto rdx = [&](int i, int tt) { fx[i & 1][tt] = lds_ld<s8>(ax[i & 3] + ((i >> 2) & 1) * 32768 + tt * 8192); };
  auto rdw = [&](int i, int ft) {
    const unsigned a = aw[ft] + ((i >> 2) & 1) * 32768 + (i & 3) * 4096;
    fw[i & 1][ft] = cat(tr_ld(a), tr_ld(a + 1024));
  };
  auto mm = [&](int i, int ft, int tt) {
    SCHED();
    acc[tt][ft] = mfma32(fw[i & 1][ft], fx[i & 1][tt], acc[tt][ft]);
    asm volatile("" : "+v"(acc[tt][ft]));
    SCHED();
  };
  wait_prog(lprog, 1, cL);
#pragma unroll
  for (int i = 0; i < 2; i++) { rdw(i, 0); rdx(i, 0); rdx(i, 1); rdw(i, 1); }
  unsigned pv = 0x80808080u;
#pragma unroll
  for (int i = 0; i < 30; i++) {
    mm(i, 0, 0);
    if ((i & 3) == 2) {
      const int s = i >> 2;
      cL = __builtin_amdgcn_readfirstlane(pv);
      SCHED();
      set_prog(myprog, s + 2);
      hook(s);
      wait_prog(lprog, s + 2, cL);
      if (s & 1) {
        asm volatile("v_xor_b32 %0, 0x10000, %0\n\tv_xor_b32 %1, 0x10000, %1\n\tv_xor_b32 %2, 0x10000, %2\n\t"
                     "v_xor_b32 %3, 0x10000, %3\n\tv_xor_b32 %4, 0x10000, %4\n\tv_xor_b32 %5, 0x10000, %5"
                     : "+v"(aw[0]), "+v"(ax[0]), "+v"(aw[1]), "+v"(ax[1]), "+v"(ax[2]), "+v"(ax[3]));
      }
    }
    if ((i & 3) == 1 && i < 28) pv = *(volatile LAS unsigned*)lprog;
    mm(i, 0, 1);
    rdw(i + 2, 0);
    mm(i, 1, 0);
    rdx(i + 2, 0);
    mm(i, 1, 1);
    rdx(i + 2, 1); rdw(i + 2, 1);
  }
  set_prog(myprog, 9);
  mm(30, 0, 0); mm(30, 1, 0);
  hook(20);
  mm(31, 0, 0); mm(31, 1, 0);
  hook(21);
  mm(30, 0, 1); mm(30, 1, 1);
  hook(22);
  mm(31, 0, 1); mm(31, 1, 1);
  hook(23);
  SCHED();
}

__global__ __launch_bounds__(1024, 4) void fused_kernel(
    const u16* __restrict__ x, const u16* __restrict__ logA, const u16* __restrict__ Aim, const u16* __restrict__ Cre,
    const u16* __restrict__ Cim, const u16* __restrict__ logdt, const u16* __restrict__ Dsk, const u16* __restrict__ w1,
    const u16* __restrict__ w2, u16* __restrict__ out, char* ws) {
  extern __shared__ __attribute__((aligned(16))) char smem[];
  {
    u64 pc0;
    asm volatile("s_getpc_b64 %0" : "=s"(pc0));
    const unsigned t0 = __builtin_amdgcn_workitem_id_x(), b0 = __builtin_amdgcn_workgroup_id_x();
    const unsigned co = umin((b0 >> 3) * 8 + (t0 & 7), kCodeLines - 1) * 128;
    asm volatile("v_cmp_gt_u32 vcc, 64, %3\n\ts_mov_b32 m0, %2\n\ts_cbranch_vccz 1f\n\tglobal_load_lds_dword %0, %1\n1:"
                 ::"v"(co), "s"(pc0), "s"(LD_DUMP), "v"(t0) : "memory", "vcc");
    SCHED();
  }
  const unsigned lds0 = (unsigned)(size_t)(LAS char*)smem;
  const int tid = __builtin_amdgcn_workitem_id_x(), lane = tid & 63;
  const int w = __builtin_amdgcn_readfirstlane(tid >> 6), role = w >> 2, rw = w & 3;
  const int b = __builtin_amdgcn_workgroup_id_x(), xcd = b & 7, idx = b >> 3;
  const int fh = xcd & 1, pl = idx & 3, cb = idx >> 2;
  const bool has1 = cb < 7;
  const int panel = 4 * (xcd >> 1) + pl, slice = 7 * fh + cb;
  const int tb = 2 * panel + fh;
  const rsrc_t rs = __builtin_amdgcn_make_buffer_rsrc(ws, 0, WS_END, 0x00020000);
  const int hh = lane >> 5, blk = (lane >> 4) & 1, q = (lane & 15) >> 2, pp = lane & 3, g = lane >> 4, n16 = lane & 15, n32 = lane & 31;
  const unsigned lprog = lds0 + LD_LPROG, aprog = lds0 + LD_APROG, bprog = lds0 + LD_BPROG, xprog = lds0 + LD_XPROG;
  unsigned cL = 0x80808080u, cA = 0x80808080u, cB = 0x80808080u, cX = 0x80808080u;
  unsigned pA, pI, pCr, pCi, ep;
  v4u i0, i1, i2, i3;
  lds_st<unsigned>(lds0 + LD_CTL + (tid & 7) * 4, (tid & 4) ? 0u : 0x80808080u);
  {
    const unsigned po = (unsigned)((2 * b * N_ + 64 * rw + lane) * 2);
    const unsigned ro = (unsigned)((64 * tb + 8 * cb + 2 * rw + hh) * 1024 + n32 * 16);
    const unsigned to = (unsigned)((64 * tb + 32 * (rw >> 1) + (lane >> 2)) * 1024 + 128 * cb + 64 * (rw & 1) + (lane & 3) * 16);
    asm volatile(
        "s_cmp_ge_u32 %[role], 2\n\t"
        "s_cbranch_scc1 2f\n\t"
        "s_cmp_eq_u32 %[role], 1\n\t"
        "s_cbranch_scc1 1f\n\t"
        "global_load_dword %[ep], %[z], %[ws] sc1\n\t"
        "global_load_ushort %[a0], %[po], %[pA]\n\t"
        "global_load_ushort %[a1], %[po], %[pI]\n\t"
        "global_load_ushort %[a2], %[po], %[pCr]\n\t"
        "global_load_ushort %[a3], %[po], %[pCi]\n\t"
        "s_branch 2f\n"
        "1:\n\t"
        "global_load_dwordx4 %[i2], %[to], %[x]\n\t"
        "global_load_dwordx4 %[i3], %[t1], %[x]\n\t"
        "global_load_dwordx4 %[i0], %[ro], %[x]\n\t"
        "global_load_dwordx4 %[i1], %[ro], %[x] offset:512\n"
        "2:"
        : [i0] "=&v"(i0), [i1] "=&v"(i1), [i2] "=&v"(i2), [i3] "=&v"(i3), [a0] "=&v"(pA), [a1] "=&v"(pI), [a2] "=&v"(pCr), [a3] "=&v"(pCi),
          [ep] "=&v"(ep)
        : [role] "s"(role), [po] "v"(po), [ro] "v"(ro), [to] "v"(to), [t1] "v"(to + 16384), [x] "s"(x), [pA] "s"(logA), [pI] "s"(Aim),
          [pCr] "s"(Cre), [pCi] "s"(Cim), [ws] "s"(ws), [z] "v"(0u), "s"(w1), "s"(w2), "s"(out), "s"(logdt), "s"(Dsk)
        : "memory", "scc");
  }
  auto get_tag = [&]() {
    v2u t;
#pragma unroll 1
    for (unsigned i = 0; i < kLdsSpin; ++i) {
      t = *(volatile LAS v2u*)(lds0 + LD_TAG);
      if (__builtin_amdgcn_readfirstlane(t.y)) break;
    }
    return (unsigned)__builtin_amdgcn_readfirstlane(t.x);
  };
#define ROLE_END() do { __builtin_amdgcn_endpgm(); __builtin_unreachable(); } while (0)
#define START_BARRIER() asm volatile("s_waitcnt lgkmcnt(0)\n\ts_barrier" ::: "memory")

  if (__builtin_expect(role == 2, 1)) {
    __builtin_amdgcn_s_setprio(2);
    const unsigned myprog = lprog + rw;
    const rsrc_t rw2 = __builtin_amdgcn_make_buffer_rsrc((void*)w2, 0, F_ * D_ * 2, 0x00020000);
    const unsigned wo2 = (unsigned)((lane >> 3) * 1024 + (((lane & 7) ^ (((lane >> 4) & 1) << 2)) << 4));
    const int wb2 = (896 * fh + 32 * rw) * 1024 + cb * 128;
    auto w2_slice = [&](int u) { int sg = u + 2 * pl + 2; sg = sg >= 7 ? sg - 7 : sg; return sg >= 7 ? sg - 7 : sg; };
    v4u wq[2][4];
    auto ldw2 = [&](int u) {
#pragma unroll
      for (int j = 0; j < 4; j++) wq[u - 5][j] = ldp128(rw2, wo2, wb2 + w2_slice(u) * 131072 + j * 8192);
      CFENCE();
    };
    auto issue_w2 = [&](int u) {
#pragma unroll
      for (int j = 0; j < 4; j++) glds(wo2, (const char*)w2 + wb2 + w2_slice(u) * 131072 + j * 8192, lds0 + u * 16384 + rw * 4096 + j * 1024);
    };
    if (__builtin_expect(has1, 1)) {
      const int rot1 = (cb + 2 * pl) & 7;
      unsigned xo[2];
#pragma unroll
      for (int j = 0; j < 2; j++) xo[j] = (unsigned)((8 * j + (lane >> 3)) * 1024 + (((lane & 7) ^ ((4 * j + g) & 7)) << 4));
      const unsigned wo = (unsigned)(g * 3584 + ((n16 ^ (g << 2)) << 4));
      const char* xa = (const char*)x + (size_t)((tb ^ 1) * 64 + 16 * rw) * 1024;
      const char* xb = (const char*)x + (size_t)(tb * 64 + 16 * rw) * 1024;
      const char* wb = (const char*)w1 + (size_t)(16 * rw) * 3584 + slice * 256;
      auto issue_x = [&](int s, int grp) {
        const int ss = (s + rot1) & 7;
#pragma unroll
        for (int j = 0; j < 2; j++) glds(xo[j], (grp ? xb : xa) + ss * 128, lds0 + (s & 3) * 32768 + grp * 8192 + rw * 2048 + j * 1024);
      };
      auto issue_w = [&](int s) {
        const int ss = (s + rot1) & 7;
#pragma unroll
        for (int j = 0; j < 4; j++) glds(wo, wb + (size_t)(ss * 64 + 4 * j) * 3584, lds0 + (s & 3) * 32768 + 16384 + rw * 4096 + j * 1024);
      };
      START_BARRIER();
      issue_x(0, 0); issue_w(0); issue_x(0, 1);
#define WAITN(n) asm volatile("s_waitcnt vmcnt(%0)" ::"i"(n) : "memory")
#pragma unroll
      for (int s = 1; s < 8; s++) {
        const int extra = s == 4 ? 8 : 0;
        if (s >= 4 && !prog_ok(cA, s - 2)) {
          WAITN(extra);
          set_prog(myprog, s);
          wait_prog(aprog, s - 2, cA);
        }
        issue_x(s, 0); issue_w(s); issue_x(s, 1);
        unsigned pv = 0;
        if (s >= 3) pv = *(volatile LAS unsigned*)aprog;
        WAITN(8 + extra);
        if (s >= 3) cA = __builtin_amdgcn_readfirstlane(pv);
        SCHED();
        set_prog(myprog, s);
        if (s == 3) { ldw2(5); ldw2(6); }
      }
      if (prog_ok(cA, 6)) {
        issue_w2(0);
        WAITV(4);
        set_prog(myprog, 8);
      } else {
        WAITV(0);
        set_prog(myprog, 8);
        wait_prog(aprog, 6, cA);
        issue_w2(0);
      }
    } else {
      const int u = (xcd >> 1) * 4 + pl, unit = 14 * fh + umin(u, 13);
      const rsrc_t rw1 = __builtin_amdgcn_make_buffer_rsrc((void*)w1, 0, F_ * D_ * 2, 0x00020000);
      v4u t[16];
      START_BARRIER();
#pragma unroll
      for (int j = 0; j < 16; j++) t[j] = ldp128(rw1, (128 * rw + (lane >> 3)) * 3584 + unit * 128 + (lane & 7) * 16, j * 8 * 3584);
      float a[8];
      unsigned sel_lo = 0x00003f80u, sel_hi = 0x3f800000u;
      asm volatile("" : "+v"(sel_lo), "+v"(sel_hi));
#pragma unroll
      for (int e = 0; e < 8; e++) a[e] = 0.f;
#pragma unroll
      for (int j = 0; j < 16; j++)
#pragma unroll
        for (int e = 0; e < 4; e++) { a[2 * e] = dot2(t[j][e], sel_lo, a[2 * e]); a[2 * e + 1] = dot2(t[j][e], sel_hi, a[2 * e + 1]); }
      lds_st<f4>(lds0 + rw * 2048 + lane * 32, f4{a[0], a[1], a[2], a[3]});
      lds_st<f4>(lds0 + rw * 2048 + lane * 32 + 16, f4{a[4], a[5], a[6], a[7]});
      set_prog(myprog, 1);
      wait_prog(lprog, 1, cL);
      float sm = 0.f;
#pragma unroll
      for (int j = 0; j < 8; j++) sm += lds_ld<float>(lds0 + (8 * g + j) * 256 + (16 * rw + n16) * 4);
      sm = add_x32(add_x16(sm));
      set_prog(myprog, 2);
      const unsigned tag = get_tag();
      st64(rs, v2u{f2u(sm), tag}, (lane < 16 && u < 14) ? lane * 8 : OOB, WS_CSUM + (unit * 64 + 16 * rw) * 8);
      wait_prog(lprog, 2, cL);
      ldw2(5); ldw2(6);
      issue_w2(0);
    }
    __builtin_amdgcn_s_setprio(1);
#pragma unroll 1
    for (int u = 1; u < 5; u++) {
      wait_prog(aprog, 6 + (u >> 1), cA);
      issue_w2(u);
    }
    set_prog(myprog, 11);
    wait_prog(aprog, 9, cA);
    asm volatile("" : "+v"(wq[0][0]), "+v"(wq[0][1]), "+v"(wq[0][2]), "+v"(wq[0][3]), "+v"(wq[1][0]), "+v"(wq[1][1]), "+v"(wq[1][2]), "+v"(wq[1][3]));
#pragma unroll
    for (int u = 5; u < 7; u++)
#pragma unroll
      for (int j = 0; j < 4; j++) lds_st<v4u>(lds0 + u * 16384 + rw * 4096 + j * 1024 + lane * 16, wq[u - 5][j]);
    WAITV(0);
    set_prog(myprog, 15);
    ROLE_END();
  } else if (role == 0) {
    if (has1) __builtin_amdgcn_s_setprio(3);
    const unsigned myprog = xprog + rw, pmask = 0x80u << (8 * (rw ^ 1));
    const int cp = b, c = rw >> 1, mh = rw & 1, ch = 2 * cp + c;
    unsigned sdt, sD;
    asm volatile("s_waitcnt lgkmcnt(0)\n\ts_load_dword %0, %2, %4\n\ts_load_dword %1, %3, %4\n\ts_barrier"
                 : "=&s"(sdt), "=&s"(sD) : "s"(logdt), "s"(Dsk), "s"(b * 4) : "memory");
    asm volatile("s_waitcnt vmcnt(4)" : "+v"(ep)::"memory");
    lds_st<v2u>(lds0 + (rw == 0 ? LD_TAG : LD_DUMP), v2u{ep + 1u, 1u});
    asm volatile("s_waitcnt lgkmcnt(0)" : "+s"(sdt), "+s"(sD)::"memory");
    const unsigned pdt = c ? sdt >> 16 : sdt & 0xffffu, pD = c ? sD >> 16 : sD & 0xffffu;
    asm volatile("s_waitcnt vmcnt(0)" : "+v"(pA), "+v"(pI), "+v"(pCr), "+v"(pCi)::"memory");
    const unsigned scr = lds0 + LD_KT + c * KT_CH + mh * KT_COPY;
    const unsigned oscr = lds0 + LD_KT + c * KT_CH + (mh ^ 1) * KT_COPY;
    f4 kd[3];
    {
      const float dt = rbf(__builtin_amdgcn_exp2f(bflo(pdt) * kLog2e));
      const float lr = -rbf(__builtin_amdgcn_exp2f(bflo(pA) * kLog2e)) * dt * kLog2e;
      const float rev = bflo(pI) * dt * 0.15915494309189535f;
      cpx C = {bflo(pCr) * dt, bflo(pCi) * dt};
      const unsigned eb = umin(umax(wave_umax(umax(f2u(C.r) & 0x7fffffffu, f2u(C.i) & 0x7fffffffu)) >> 23, 12u), 254u);
      const float sdn = u2f((266u - eb) << 23), sup = u2f((eb - 11u) << 23);
      C.r *= sdn; C.i *= -sdn;
      const cpx e8 = cexp_at(lr, rev, 8.f);
      const cpx e1 = cexp_at(lr, -rev, 1.f), e2 = cmul(e1, e1), e4 = cmul(e2, e2), gg = cexp_at(lr, -rev, 128.f);
      const unsigned wa = scr + lane * 4, ra = scr + n16 * 272 + g * 16;
      auto put = [&](int i, cpx v) { lds_st<unsigned>(wa + i * 272, pkh(v.r, v.i)); };
      v4u fq[4];
      {
        const cpx e16 = cmul(e8, e8);
        cpx a = e16, bq = cmul(e8, e16);
        put(0, cpx{1.f, 0.f}); put(1, e8);
#pragma unroll
        for (int j = 2; j < 16; j += 2) {
          put(j, a); put(j + 1, bq);
          if (j < 14) { a = cmul(a, e16); bq = cmul(bq, e16); }
        }
        CFENCE();
#pragma unroll
        for (int s = 0; s < 4; s++) fq[s] = lds_ld<v4u>(ra + s * 64);
        CFENCE();
      }
      auto tile = [&](int T) {
        CFENCE();
        v4u fp[4];
#pragma unroll
        for (int s = 0; s < 4; s++) fp[s] = lds_ld<v4u>(ra + s * 64);
        CFENCE();
        f4 a = {0.f, 0.f, 0.f, 0.f};
#pragma unroll
        for (int s = 0; s < 4; s++) a = mfma16h(fp[s], fq[s], a);
        kd[T] = a * sup;
      };
      cpx P = C;
      {
        cpx a = P, bq = cmul(P, gg);
        P = cmul(bq, gg);
#pragma unroll
        for (int j = 0; j < 8; j++) {
          put(j, a); put(8 + j, bq);
          if (j < 7) { a = cmul(a, e1); bq = cmul(bq, e1); }
        }
      }
      tile(0);
      const cpx P2 = P, P3 = cmul(P, gg), P4 = cmul(P3, gg);
      P = P4;
#pragma unroll
      for (int j = 0; j < 12; j++) {
        put(4 * (j & 3) + (j < 4 ? 0 : 1 + (j >> 2)), P);
        if (j < 4) put(4 * j + 1, cmul(P, e4));
        P = cmul(P, gg);
      }
      tile(1);
      {
        cpx a = P2, bq = P3;
#pragma unroll
        for (int j = 0; j < 4; j++) {
          put(j, a); put(4 + j, bq);
          if (j < 3) { a = cmul(a, e2); bq = cmul(bq, e2); }
        }
        put(8, P4); put(9, P);
      }
      tile(2);
    }
    const unsigned ut = lds0 + LD_UT + c * UT_CH, kt = lds0 + LD_KT + c * KT_CH;
    if (mh == 0) {
      lds_st<f4>(scr + 1024 + lane * 16, kd[1]);
      lds_st<float>(scr + 2048 + lane * 4, kd[2][1]);
      set_prog(myprog, 2);
      wait_prog(xprog, 2, cX, pmask);
      f4 v = kd[0] + lds_ld<f4>(oscr + 3072 + lane * 16);
      const f4 u = kd[2] + lds_ld<f4>(oscr + 4096 + lane * 16);
      CFENCE();
      v[0] += lane == 0 ? bflo(pD) : 0.f;
      const float nx = u2f(__builtin_amdgcn_update_dpp(0u, f2u(u[0]), 0x130, 0xf, 0xf, false));
      const unsigned a1 = lane < 32 ? kt + 3568 - 16 * lane : lds0 + LD_DUMP;
      const unsigned u22 = pkbf(u[2], u[2]), u00 = pkbf(u[0], u[0]);
      lds_st<v4u>(a1, v4u{pkbf(nx, u[3]), u22, pkbf(u[2], u[1]), u00});
      lds_st<v4u>(lane < 32 ? a1 + KT_COPY : a1, v4u{pkbf(u[3], u[2]), u22, pkbf(u[1], u[0]), u00});
      CFENCE();
      const unsigned a0 = kt + 4088 - 2 * (128 * (g >> 1) + 8 * n16 + 4 * (g & 1));
      const unsigned v32 = pkbf(v[3], v[2]), v10 = pkbf(v[1], v[0]);
      lds_st<v2u>(a0, v2u{v32, v10});
      lds_st<u16>(a0 + KT_COPY - 2, (u16)v32);
      lds_st<unsigned>(a0 + KT_COPY, pkbf(v[2], v[1]));
      lds_st<u16>(a0 + KT_COPY + 4, (u16)(v10 >> 16));
      lds_st<u16>(kt + KT_COPY + 4094, (u16)0);
      lds_st<unsigned>(kt + 4096 + (lane & 7) * 4 + ((lane >> 3) & 1) * KT_COPY, 0u);
    }
    if (opaque(mh) != 0) {
      lds_st<f4>(scr + 3072 + lane * 16, kd[0]);
      lds_st<f4>(scr + 4096 + lane * 16, kd[2]);
      set_prog(myprog, 2);
      wait_prog(xprog, 2, cX, pmask);
      const f4 v = kd[1] + lds_ld<f4>(oscr + 1024 + lane * 16);
      const float kend = u2f(__builtin_amdgcn_readlane(f2u(kd[2][1]), 32)) + lds_ld<float>(oscr + 2048 + 128);
      CFENCE();
      auto next = [&](float cur, unsigned last) { return u2f(__builtin_amdgcn_update_dpp(last, f2u(cur), 0x130, 0xf, 0xf, false)); };
      auto rol = [&](float t) { return __builtin_amdgcn_update_dpp(0u, f2u(t), 0x134, 0xf, 0xf, false); };
      const float n0 = next(v[0], rol(v[2])), n2 = next(v[2], rol(v[3])), n3 = next(v[3], f2u(kend));
      const unsigned a1 = kt + 1008 - 16 * lane;
      {
        const unsigned v11 = pkbf(v[1], v[1]), v00 = pkbf(v[0], v[0]);
        lds_st<v4u>(a1 + 2048, v4u{pkbf(n0, n0), v11, pkbf(v[1], v[0]), v00});
        lds_st<v4u>(a1 + 2048 + KT_COPY, v4u{pkbf(n0, v[1]), v11, v00, v00});
      }
#pragma unroll
      for (int r = 2; r < 4; r++) {
        const float nx = r == 2 ? n2 : n3, z = (lane & 1) ? nx : v[r];
        const unsigned nn = pkbf(nx, nx), vv = pkbf(v[r], v[r]);
        lds_st<v4u>(a1 + (3 - r) * 1024, v4u{nn, pkbf(nx, z), vv, vv});
        lds_st<v4u>(a1 + (3 - r) * 1024 + KT_COPY, v4u{nn, pkbf(z, v[r]), vv, vv});
      }
    }
    set_prog(myprog, 5);
    const unsigned tag = get_tag();
    st_preflag(rs, tag, lane < 4 ? lane * 16 : OOB, WS_YFLAG + (mh * 256 + cp) * 128 + c * 64);
    wait_prog(xprog, 5, cX, pmask);
    wait_prog(bprog, 2, cB);
    wait_prog(aprog, 9, cA);
    __builtin_amdgcn_s_setprio(3);
    f4 ya[8];
#pragma unroll
    for (int i = 0; i < 8; i++) ya[i] = f4{0.f, 0.f, 0.f, 0.f};
    {
      const unsigned ka = lds0 + LD_KT + c * KT_CH + (1 - (n16 & 1)) * KT_COPY + 4 * (1015 - (n16 >> 1) + 4 * g) - 64 * (59 + 4 * mh);
      const unsigned ua = ut + 2 * (224 + 16 * n16 + 8 * g) - 64 * (3 + 4 * mh);
      unsigned kb[4];
#pragma unroll
      for (int j = 0; j < 4; j++) { kb[j] = ka + 1024 * j; asm volatile("" : "+v"(kb[j])); }
      typedef unsigned v2a __attribute__((ext_vector_type(2), aligned(4)));
      unsigned pvx;
      v2u kl[2][8], kh_[2][8];
      s8 uf[2][8];
#define KL(r, k) kl[(r) & 1][k] = *(LAS v2a*)(kb[(7 - (k)) >> 1] + 64 * (3 - (r)) + 512 * ((7 - (k)) & 1))
#define KH(r, k) kh_[(r) & 1][k] = *(LAS v2a*)(kb[(7 - (k)) >> 1] + 64 * (3 - (r)) + 512 * ((7 - (k)) & 1) + 8)
#define UL(r, e) uf[(r) & 1][e] = lds_ld<s8>(ua + 64 * (3 - (r)) + 512 * (e))
#define MM(r, k, e) SCHED(); ya[(k) + (e)] = mfma16(__builtin_bit_cast(s8, v4u{kl[(r) & 1][k].x, kl[(r) & 1][k].y, kh_[(r) & 1][k].x, kh_[(r) & 1][k].y}), uf[(r) & 1][e], ya[(k) + (e)]); SCHED()
      KL(0, 0); KH(0, 0); UL(0, 7); UL(0, 6); UL(0, 5); UL(0, 4); UL(0, 3); UL(0, 2); UL(0, 1); UL(0, 0); KL(0, 1); KH(0, 1);
      MM(0, 0, 7); MM(0, 0, 6); MM(0, 0, 5); MM(0, 0, 4); MM(0, 0, 3); KL(0, 2); MM(0, 0, 2); KH(0, 2); MM(0, 0, 1); MM(0, 0, 0);
      MM(0, 1, 6); MM(0, 1, 5); MM(0, 1, 4); KL(0, 3); MM(0, 1, 3); KH(0, 3); MM(0, 1, 2); MM(0, 1, 1); MM(0, 1, 0);
      MM(0, 2, 5); KL(0, 4); MM(0, 2, 4); KH(0, 4); MM(0, 2, 3); KL(0, 5); MM(0, 2, 2); KH(0, 5); MM(0, 2, 1); KL(0, 6);
      MM(0, 2, 0); KH(0, 6); MM(0, 3, 4); KL(0, 7); MM(0, 3, 3); KH(0, 7); MM(0, 3, 2); KL(1, 0); MM(0, 3, 1); KH(1, 0);
      MM(0, 3, 0); UL(1, 7); MM(0, 4, 3); UL(1, 6); MM(0, 4, 2); KL(1, 1); MM(0, 4, 1); KH(1, 1); MM(0, 4, 0); UL(1, 5);
      MM(0, 5, 2); KL(1, 2); MM(0, 5, 1); KH(1, 2); MM(0, 5, 0); UL(1, 4); MM(0, 6, 1); MM(0, 6, 0); KL(1, 3); MM(0, 7, 0); KH(1, 3);
      MM(1, 0, 7); UL(1, 3); MM(1, 0, 6); MM(1, 1, 6); MM(1, 0, 5); KL(1, 4); MM(1, 1, 5); KH(1, 4); MM(1, 2, 5); UL(1, 2); MM(1, 0, 4);
      MM(1, 1, 4); MM(1, 2, 4); MM(1, 3, 4); KL(1, 5); MM(1, 0, 3); KH(1, 5); MM(1, 1, 3); UL(1, 1); MM(1, 2, 3); MM(1, 3, 3);
      MM(1, 4, 3); MM(1, 0, 2); MM(1, 1, 2); KL(1, 6); MM(1, 2, 2); KH(1, 6); MM(1, 3, 2); UL(1, 0); MM(1, 4, 2); MM(1, 5, 2);
      MM(1, 0, 1); KL(1, 7); MM(1, 1, 1); KH(1, 7); MM(1, 2, 1); KL(2, 0); MM(1, 3, 1); KH(2, 0); MM(1, 4, 1); UL(2, 7);
      MM(1, 5, 1); UL(2, 6); MM(1, 6, 1); UL(2, 5); MM(1, 0, 0); UL(2, 4); MM(1, 1, 0); UL(2, 3); MM(1, 2, 0); UL(2, 2);
      MM(1, 3, 0); UL(2, 1); MM(1, 4, 0); UL(2, 0); MM(1, 5, 0); KL(2, 1); MM(1, 6, 0); KH(2, 1); MM(1, 7, 0); MM(2, 0, 7); MM(2, 0, 6);
      MM(2, 0, 5); MM(2, 0, 4); MM(2, 0, 3); KL(2, 2); MM(2, 0, 2); KH(2, 2); MM(2, 0, 1); MM(2, 0, 0); MM(2, 1, 6); MM(2, 1, 5);
      MM(2, 1, 4); KL(2, 3); MM(2, 1, 3); KH(2, 3); MM(2, 1, 2); MM(2, 1, 1); MM(2, 1, 0); MM(2, 2, 5); KL(2, 4); MM(2, 2, 4); KH(2, 4);
      MM(2, 2, 3); KL(2, 5); MM(2, 2, 2); KH(2, 5); MM(2, 2, 1); KL(2, 6); MM(2, 2, 0); KH(2, 6); MM(2, 3, 4); KL(2, 7);
      MM(2, 3, 3); KH(2, 7); MM(2, 3, 2); KL(3, 0); MM(2, 3, 1); KH(3, 0); MM(2, 3, 0); UL(3, 7); MM(2, 4, 3); UL(3, 6);
      MM(2, 4, 2); KL(3, 1); MM(2, 4, 1); KH(3, 1); MM(2, 4, 0); UL(3, 5); MM(2, 5, 2); KL(3, 2); MM(2, 5, 1); KH(3, 2);
      MM(2, 5, 0); UL(3, 4); MM(2, 6, 1); MM(2, 6, 0); KL(3, 3); MM(2, 7, 0); KH(3, 3); MM(3, 0, 7); UL(3, 3); MM(3, 0, 6); MM(3, 1, 6);
      MM(3, 0, 5); KL(3, 4); MM(3, 1, 5); KH(3, 4); MM(3, 2, 5); UL(3, 2); MM(3, 0, 4); MM(3, 1, 4); MM(3, 2, 4); MM(3, 3, 4); KL(3, 5);
      MM(3, 0, 3); KH(3, 5); MM(3, 1, 3); UL(3, 1); MM(3, 2, 3); MM(3, 3, 3); MM(3, 4, 3); MM(3, 0, 2); MM(3, 1, 2); KL(3, 6);
      MM(3, 2, 2); KH(3, 6); MM(3, 3, 2); UL(3, 0); MM(3, 4, 2); MM(3, 5, 2); MM(3, 0, 1); MM(3, 1, 1); MM(3, 2, 1);
      MM(3, 3, 1); KL(3, 7); MM(3, 4, 1); KH(3, 7); set_prog(myprog, 6); MM(3, 5, 1); MM(3, 6, 1); MM(3, 0, 0); MM(3, 1, 0);
      MM(3, 2, 0); MM(3, 3, 0); pvx = *(volatile LAS unsigned*)xprog; MM(3, 4, 0); MM(3, 5, 0); MM(3, 6, 0); MM(3, 7, 0);
#undef KL
#undef KH
#undef UL
#undef MM
      cX = __builtin_amdgcn_readfirstlane(pvx);
      SCHED();
    }
    {
      wait_prog(xprog, 6, cX, pmask);
      auto finish = [&](int give, int keep) {
#pragma unroll
        for (int j = 0; j < 4; j++) lds_st<f4>(scr + j * 1024 + lane * 16, ya[give + j]);
        set_prog(myprog, 7);
        int yo = (256 * (g & 1) + 16 * n16 + 8 * (g >> 1)) * 2;
        unsigned ra = oscr + lane * 16;
        asm volatile("" : "+v"(yo), "+v"(ra));
        wait_prog(xprog, 7, cX, pmask);
        f4 o[4];
#pragma unroll
        for (int j = 0; j < 4; j++) o[j] = lds_ld<f4>(ra + j * 1024);
        set_prog(myprog, 8);
        SCHED();
#pragma unroll
        for (int z = 0; z < 2; z++) {
          const f4 u = ya[keep + 2 * z] + o[2 * z], v = ya[keep + 2 * z + 1] + o[2 * z + 1];
          const auto r0 = __builtin_amdgcn_permlane16_swap(pkbf(u[0], u[1]), pkbf(v[0], v[1]), false, false);
          const auto r1 = __builtin_amdgcn_permlane16_swap(pkbf(u[2], u[3]), pkbf(v[2], v[3]), false, false);
          const unsigned e0 = r0[0], e1 = r1[0], e2 = r0[1], e3 = r1[1];
          st128(rs, v4u{e0, e1, e2, e3}, yo + 1024 * z, WS_YT + ch * 4096 + mh * 2048);
        }
      };
      if (mh == 0) finish(4, 0);
      if (opaque(mh) != 0) finish(0, 4);
      st_flag(rs, tag, lane, 4, WS_YFLAG + (mh * 256 + cp) * 128 + c * 64);
    }
    {
      wait_prog(xprog, 6, cX);
      int yo = (lane >> 3) * 4096 + (lane & 7) * 16, ys = WS_YT + (64 * cb + 16 * rw) * 4096 + tb * 128, ys1 = ys + 32768;
      unsigned yd = lds0 + LD_UT + (16 * rw + (lane >> 3)) * 128 + (((lane & 7) ^ (((lane >> 4) & 1) << 2)) << 4);
      asm volatile("" : "+v"(yo), "+v"(yd));
      asm volatile("" : "+s"(ys), "+s"(ys1));
      __builtin_amdgcn_s_sleep(40);
      wait_tag<6>(rs, WS_YFLAG + ((tb >> 4) * 256 + 32 * cb + 8 * rw) * 128 + (lane & 15) * 64, tag);
      const v4u y0 = ld128(rs, yo, ys), y1 = ld128(rs, yo, ys1);
      lds_st<v4u>(yd, y0);
      lds_st<v4u>(yd + 1024, y1);
      set_prog(myprog, 9);
    }
    ROLE_END();
  } else {
    const bool isA = role != 1;
    __builtin_amdgcn_s_setprio(3);
    const unsigned gprog = isA ? aprog : bprog, myprog = gprog + rw, pmask = 0x80u << (8 * (rw ^ 1));
    const int th = rw >> 1, kh = rw & 1;
    const int tbk = tb ^ (role >> 1);
    unsigned cG = 0x80808080u;
    START_BARRIER();
    const unsigned tag = get_tag();
    constexpr int WIN = 3;
    v4u hf[WIN][4];
    v2u xres[4];
#pragma unroll
    for (int j = 0; j < 4; j++) asm volatile("" : "=v"(xres[j]));
    const int hb = WS_H + ((2 * tbk + th) * 112 + 56 * fh + 4 * kh) * 1024;
    const int hfl = WS_HFLAG + ((tbk * 2 + fh) * 4 + rw) * 1024;
    const unsigned hx = lds0 + 7 * 16384 + rw * 4096 + lane * 16;
#pragma unroll
    for (int s = 0; s < 4; s++) asm volatile("" : "=v"(hf[0][s]));
    if (!isA) {
      asm volatile("s_waitcnt vmcnt(2)" : "+v"(i2), "+v"(i3)::"memory");
      const unsigned scr = lds0 + LD_UT + rw * 2048;
      lds_st<v4u>(scr + lane * 16, i2);
      lds_st<v4u>(scr + 1024 + lane * 16, i3);
      CFENCE();
      v4u tr[2];
#pragma unroll
      for (int j = 0; j < 2; j++) {
        const unsigned a = scr + (8 * g + q) * 64 + 32 * j + 8 * pp;
        tr[j] = __builtin_bit_cast(v4u, cat(tr_ld(a), tr_ld(a + 256)));
      }
#pragma unroll
      for (int j = 0; j < 4; j++) xres[j] = lds_ld<v2u>(scr + n32 * 64 + 16 * j + 8 * hh);
      set_prog(myprog, 1);
      asm volatile("s_waitcnt vmcnt(0)" : "+v"(i0), "+v"(i1)::"memory");
#pragma unroll
      for (int j = 0; j < 2; j++)
        st128(rs, tr[j], (n16 >> 1) * 8192 + (n16 & 1) * 64 + g * 16, WS_XT + (32 * cb + 16 * kh + 8 * j) * 8192 + (2 * tb + th) * 128);
      float s1 = 0.f, s2 = 0.f;
#pragma unroll
      for (int j = 0; j < 4; j++) {
        s1 = dot2(i1[j], 0x3f803f80u, dot2(i0[j], 0x3f803f80u, s1));
        s2 = dot2(i1[j], i1[j], dot2(i0[j], i0[j], s2));
      }
      const float m = add_x16(sum16(s1)) * (1.f / D_);
      const float var = rbf(__builtin_fmaxf(add_x16(sum16(s2)) * (1.f / D_) - m * m, 0.f));
      const unsigned gv = pkbf(m, __builtin_amdgcn_sqrtf(rbf(var + 1e-5f)));
      const int sbk = 8 * tb + cb;
      st64(rs, v2u{gv, tag}, n32 == 0 ? hh * 8 : OOB, WS_GRAN + (((sbk >> 6) * 4 + rw) * 64 + (sbk & 63)) * 16);
      st_flag(rs, tag, lane, 4, WS_XFLAG + ((2 * cb + kh) * 32 + tb) * 128 + th * 64);
      __builtin_amdgcn_s_setprio(1);
      if (has1) wait_prog(lprog, 5, cL);
      if (!has1) __builtin_amdgcn_s_sleep(40);
      wait_tag<8>(rs, WS_XFLAG + ((b >> 4) * 32 + 8 * rw) * 128 + (lane & 15) * 64, tag);
      if (has1) wait_prog(lprog, 7, cL);
      const int tok0 = 512 * rw + 8 * lane;
      v4u gq[4], xv[2];
#pragma unroll
      for (int k = 0; k < 2; k++) xv[k] = ld128(rs, (tok0 >> 5) * 128 + (lane & 3) * 16 + k * 64, WS_XT + b * 8192);
#pragma unroll 1
      for (unsigned i = 0; i < kMemSpin; ++i) {
        CFENCE();
#pragma unroll
        for (int j = 0; j < 4; j++) gq[j] = ld128(rs, WS_GRAN + lane * 16, (rw * 4 + j) * 1024);
        CFENCE();
        unsigned bad = 0;
#pragma unroll
        for (int j = 0; j < 4; j++) bad |= (gq[j].y ^ tag) | (gq[j].w ^ tag);
        if (wall(bad == 0)) break;
        __builtin_amdgcn_s_sleep(8);
      }
      float mr[8], ri[8];
#pragma unroll
      for (int j = 0; j < 4; j++) {
        ri[2 * j] = __builtin_amdgcn_rcpf(bfhi(gq[j].x)); mr[2 * j] = -bflo(gq[j].x) * ri[2 * j];
        ri[2 * j + 1] = __builtin_amdgcn_rcpf(bfhi(gq[j].z)); mr[2 * j + 1] = -bflo(gq[j].z) * ri[2 * j + 1];
      }
      wait_prog(bprog, 1, cG);
#pragma unroll
      for (int k = 0; k < 2; k++) {
        v4u o;
#pragma unroll
        for (int j = 0; j < 4; j++) o[j] = pkbf(bflo(xv[k][j]) * ri[2 * j] + mr[2 * j], bfhi(xv[k][j]) * ri[2 * j + 1] + mr[2 * j + 1]);
        lds_st<v4u>(lds0 + LD_UT + k * UT_CH + 480 + tok0 * 2, o);
      }
      if (rw == 0) lds_st<v4u>(lds0 + LD_UT + hh * UT_CH + umin(n32, 29) * 16, v4u{0u, 0u, 0u, 0u});
      if ((tb >> 3) == rw) {
        const unsigned ar = lds0 + ((tok0 >> 6) == tb ? LD_RS : LD_DUMP) + (tok0 & 63) * 4;
        lds_st<f4>(ar, f4{ri[0], ri[1], ri[2], ri[3]});
        lds_st<f4>(ar + 16, f4{ri[4], ri[5], ri[6], ri[7]});
      }
      set_prog(myprog, 2);
      if (b == 0 && rw == 0) {
        wait_prog(bprog, 2, cG);
        st32(rs, tag, lane == 0 ? 0 : OOB, WS_EPOCH);
      }
      wait_prog(aprog, 10, cA);
#pragma unroll
      for (int s = 0; s < 4; s++) hf[0][s] = lds_ld<v4u>(hx + s * 1024);
      set_prog(myprog, 3);
    }
    if (opaque(role) == 3) {
      const int o = lane < 8 ? lane * 16 : OOB;
      st_preflag(rs, tag, o, hfl + cb * 128);
      st_preflag(rs, tag, o, WS_PFLAG + (b * 4 + rw) * 128);
    }
    int role1 = has1 ? role : role + 4;
    asm volatile("" : "+s"(role1));
    if (role1 == 3) {
      f16v acc[2][2];
#pragma unroll
      for (int i = 0; i < 16; i++) acc[0][0][i] = acc[0][1][i] = acc[1][0][i] = acc[1][1][i] = 0.f;
      const Geo G = {lds0, lane, rw, hh, blk, q, pp, g, n16, n32};
      const int t = 64 * (tbk ^ hh) + 32 * th + n32;
      const int mo = WS_GRAN + ((((t >> 9) * 4 + ((t >> 1) & 3)) * 64 + ((t >> 3) & 63)) * 2 + (t & 1)) * 8;
      const int co = WS_CSUM + (slice * 128 + 64 * kh + lane) * 8;
      v2u gm, gc;
      unsigned p0, p1, ms0, ms1;
      auto prepare = [&]() {
        if (__builtin_expect(!wall(gm.y == tag && gc.y == tag), 0)) {
#pragma unroll 1
          for (unsigned i = 0; i < kMemSpin; ++i) {
            __builtin_amdgcn_s_sleep(4);
            CFENCE();
            gm = ld64(rs, mo, 0); gc = ld64(rs, co, 0);
            if (wall(gm.y == tag && gc.y == tag)) break;
          }
        }
        const unsigned mm = (gm.x ^ 0x8000u) & 0xffffu;
        const auto ms = __builtin_amdgcn_permlane32_swap(mm, mm, false, false);
        ms0 = ms[0]; ms1 = ms[1];
        const float cs = u2f(gc.x), c0 = rbf(cs), c1 = rbf(cs - c0);
        p0 = pkbf(c0, c1); p1 = pkbf(cs - c0 - c1, 0.f);
      };
      auto correct = [&](int tt) {
        const unsigned m = tt ? ms1 : ms0;
        const s8 cm = __builtin_bit_cast(s8, v4u{m | (m << 16), m, 0u, 0u});
        acc[tt][0] = mfma32(__builtin_bit_cast(s8, v4u{hh ? 0u : p0, hh ? 0u : p1, 0u, 0u}), cm, acc[tt][0]);
        acc[tt][1] = mfma32(__builtin_bit_cast(s8, v4u{hh ? p0 : 0u, hh ? p1 : 0u, 0u, 0u}), cm, acc[tt][1]);
        asm volatile("" : "+v"(acc[tt][0]), "+v"(acc[tt][1]));
      };
      const int hd = (tbk & 1) ? -2 * 112 * 1024 : 2 * 112 * 1024;
      auto put_h = [&](int tt, int s) {
#pragma unroll
        for (int j = 0; j < 4; j++) {
          const unsigned p = pkbf(acc[tt][s >> 1][8 * (s & 1) + 2 * j], acc[tt][s >> 1][8 * (s & 1) + 2 * j + 1]);
          hf[tt][s][j] = __builtin_bit_cast(unsigned, __builtin_elementwise_max(__builtin_bit_cast(sh2, p), sh2{0, 0}));
        }
        st128(rs, hf[tt][s], lane * 16 + s * 1024, hb + cb * 8192 + tt * hd);
      };
      gemm1_loop(G, acc, cL, [&](int s) {
        if (s == 6) { gm = ld64(rs, mo, 0); gc = ld64(rs, co, 0); }
        if (s == 20) prepare();
        if (s == 21) correct(0);
        if (s == 22) { put_h(0, 0); put_h(0, 1); }
        if (s == 23) { correct(1); put_h(0, 2); put_h(0, 3); SCHED(); put_h(1, 0); put_h(1, 1); put_h(1, 2); put_h(1, 3); }
      });
      wait_prog(aprog, 9, cA);
#pragma unroll
      for (int s = 0; s < 4; s++) lds_st<v4u>(hx + s * 1024, hf[1][s]);
      st_flag(rs, tag, lane, 8, hfl + cb * 128);
      set_prog(myprog, 10);
#pragma unroll
      for (int j = 0; j < 4; j++) asm volatile("" : "=v"(xres[j]));
    }
    asm volatile("" : "+s"(role1));
    if (role1 == 7) {
      set_prog(myprog, 9);
      wait_prog(lprog, 11, cL);
      set_prog(myprog, 10);
      wait_prog(xprog, 8, cX);
    }
    {
    const int lane = fresh_lane();
    int role2 = role;
    asm volatile("" : "+s"(role2));
    const bool isA = role2 != 1;
    cG = 0x80808080u;
    const int hh = lane >> 5, blk = (lane >> 4) & 1, q = (lane & 15) >> 2, pp = lane & 3, n32 = lane & 31;
    if (!isA) __builtin_amdgcn_s_setprio(1);
    int hso[7];
#pragma unroll
    for (int c = 0; c < 7; c++) {
      int sg = c + cb; sg = sg >= 7 ? sg - 7 : sg;
      hso[c] = hb + sg * 8192;
      asm volatile("" : "+s"(hso[c]));
    }
    auto ldh = [&](int c, int s) { hf[c % WIN][s] = ld128(rs, lane * 16 + s * 1024, hso[c]); };
    unsigned a2[2];
    int un = cb + 12 - 2 * pl; un = un >= 14 ? un - 14 : un; un = un >= 7 ? un - 7 : un;
#pragma unroll
    for (int ct = 0; ct < 2; ct++) a2[ct] = lds0 + un * 16384 + (64 * kh + 4 * hh + q) * 128 + ((ct ^ kh ^ 1 ^ (q >> 1)) << 6) + 32 * blk + 8 * pp;
    constexpr int NB = 3;
    s8 fw2[NB][2];
    auto rd2 = [&](int i, int ct) {
      if (ct == 0 && i > 0 && (i & 3) == 0) {
        const int d = un == 6 ? -6 * 16384 : 16384;
        un = un == 6 ? 0 : un + 1;
        asm volatile("v_add_u32 %0, %2, %0\n\tv_add_u32 %1, %2, %1" : "+v"(a2[0]), "+v"(a2[1]) : "s"(d));
      }
      const unsigned a = a2[ct] + (i & 3) * 2048;
      fw2[i % NB][ct] = cat(tr_ld(a), tr_ld(a + 1024));
    };
    wait_prog(lprog, 11, cL);
    asm volatile("" : "+v"(a2[0]), "+v"(a2[1]));
    SCHED();
    if (isA) {
      int hfo = hfl + umin(lane, 6) * 128;
      asm volatile("" : "+v"(hfo));
      __builtin_amdgcn_s_sleep(8);
      wait_tag<4>(rs, hfo, tag);
    }
    wait_prog(aprog, isA ? 0 : 13, cA);
    if (__builtin_expect(opaque(cb) == 7, 0)) {
#pragma unroll
      for (int s = 0; s < 4; s++) ldh(0, s);
    }
#pragma unroll
    for (int c = 1; c < WIN; c++)
#pragma unroll
      for (int s = 0; s < 4; s++) { ldh(c, s); SCHED(); }
    wait_prog(lprog, 15, cL);
#pragma unroll
    for (int i = 0; i < NB; i++) { rd2(i, 0); rd2(i, 1); }
    f16v ac2[2];
#pragma unroll
    for (int i = 0; i < 16; i++) ac2[0][i] = ac2[1][i] = 0.f;
    v4u pall;
    unsigned pone;
    auto mm2 = [&](int i, int t) {
      SCHED();
      ac2[t] = mfma32(fw2[i % NB][t], __builtin_bit_cast(s8, hf[(i >> 2) % WIN][i & 3]), ac2[t]);
      asm volatile("" : "+v"(ac2[t]));
      SCHED();
    };
#pragma unroll
    for (int i = 0; i < 28 - NB; i++) {
      const int c = i >> 2, s = i & 3;
      mm2(i, 0);
      rd2(i + NB, 0);
      mm2(i, 1);
      rd2(i + NB, 1);
      if (c + WIN < 7) ldh(c + WIN, s);
      if (i == 11) set_prog(myprog, 13);
      if (i == 22) pone = *(volatile LAS unsigned*)(isA ? bprog : xprog);
    }
    SCHED();
    cX = __builtin_amdgcn_readfirstlane(pone);
    wait_prog(isA ? bprog : xprog, isA ? 3 : 8, cX);
#pragma unroll
    for (int i = 28 - NB; i < 28; i++) mm2(i, 0);
#pragma unroll
    for (int i = 28 - NB; i < 28; i++) mm2(i, 1);
    const unsigned ex = (isA ? lds0 + 7 * 16384 : lds0 + LD_KT) + lane * 16;
#pragma unroll
    for (int j = 0; j < 4; j++) lds_st<f4>(ex + rw * 4096 + j * 1024, f4{ac2[0][4 * j], ac2[0][4 * j + 1], ac2[0][4 * j + 2], ac2[0][4 * j + 3]});
    set_prog(myprog, 14);
    f16v& keep = ac2[1];
    const unsigned px = lds0 + 7 * 16384 + (rw ^ 1) * 4096 + lane * 16;
    if (__builtin_expect(isA, 1)) {
      wait_prog(gprog, 14, cG, pmask);
      v4u pr[2];
      f4 o4[4];
#pragma unroll
      for (int j = 0; j < 4; j++) o4[j] = lds_ld<f4>(ex + (rw ^ 1) * 4096 + j * 1024);
      SCHED();
#pragma unroll
      for (int j = 0; j < 4; j++) {
        const f4 o = o4[j];
        pr[j >> 1][2 * (j & 1)] = pkbf(keep[4 * j] + o[0], keep[4 * j + 1] + o[1]);
        pr[j >> 1][2 * (j & 1) + 1] = pkbf(keep[4 * j + 2] + o[2], keep[4 * j + 3] + o[3]);
      }
#pragma unroll
      for (int j = 0; j < 2; j++) st128(rs, pr[j], lane * 16 + j * 1024, WS_PART + (b * 4 + rw) * 2048);
      st_flag(rs, tag, lane, 8, WS_PFLAG + (b * 4 + rw) * 128);
      int ps = WS_PART + ((b ^ 1) * 4 + rw) * 2048;
      unsigned pxa = px;
      asm volatile("" : "+s"(ps));
      asm volatile("" : "+v"(pxa));
      __builtin_amdgcn_s_sleep(10);
      wait_tag<2>(rs, WS_PFLAG + ((b ^ 1) * 4 + rw) * 128, tag);
#pragma unroll
      for (int j = 0; j < 2; j++) pr[j] = ld128(rs, lane * 16 + j * 1024, ps);
#pragma unroll
      for (int j = 0; j < 2; j++) lds_st<v4u>(pxa + j * 1024, pr[j]);
      set_prog(myprog, 15);
      ROLE_END();
    } else {
      __builtin_amdgcn_s_setprio(3);
      const int lane = fresh_lane();
      const int hh = lane >> 5, blk = (lane >> 4) & 1, q = (lane & 15) >> 2, pp = lane & 3, n32 = lane & 31;
      const float rstd = lds_ld<float>(lds0 + LD_RS + (32 * th + n32) * 4);
      unsigned ay = lds0 + LD_UT + (32 * kh + 4 * hh + q) * 128 + ((th ^ (q >> 1)) << 6) + 32 * blk + 8 * pp;
      unsigned op = (unsigned)((64 * tb + 32 * th + n32) * D_ + 64 * cb + 32 * kh + 8 * hh) * 2u;
      unsigned oa = ex + (rw ^ 1) * 4096, pa = px;
      asm volatile("" : "+v"(ay), "+v"(op), "+v"(oa), "+v"(pa));
      float base[16];
#pragma unroll
      for (int j = 0; j < 4; j++) {
        base[4 * j] = bflo(xres[j].x) + rstd * keep[4 * j]; base[4 * j + 1] = bfhi(xres[j].x) + rstd * keep[4 * j + 1];
        base[4 * j + 2] = bflo(xres[j].y) + rstd * keep[4 * j + 2]; base[4 * j + 3] = bfhi(xres[j].y) + rstd * keep[4 * j + 3];
      }
      PIN8(base, 0);
      pall = *(volatile LAS v4u*)aprog;
      PIN8(base, 8);
      cA = __builtin_amdgcn_readfirstlane(pall.x); cG = __builtin_amdgcn_readfirstlane(pall.y); cX = __builtin_amdgcn_readfirstlane(pall.w);
      wait_prog(gprog, 14, cG, pmask);
      f4 o[4];
#pragma unroll
      for (int j = 0; j < 4; j++) o[j] = lds_ld<f4>(oa + j * 1024);
      auto add_o = [&]() {
#pragma unroll
        for (int j = 0; j < 4; j++)
#pragma unroll
          for (int e = 0; e < 4; e++) base[4 * j + e] += rstd * o[j][e];
        PIN16(base);
      };
      const unsigned ymask = 0x8080u << (16 * kh);
      const bool ye = (ymask & ~(cX - 0x09090909u)) == 0;
      if (!ye) { add_o(); wait_prog(xprog, 9, cX, ymask); }
      v2u y[4];
#pragma unroll
      for (int j = 0; j < 4; j++) y[j] = __builtin_bit_cast(v2u, tr_ld(ay + j * 1024));
      const unsigned pa1 = *(volatile LAS unsigned*)aprog;
      if (ye) add_o();
#pragma unroll
      for (int j = 0; j < 4; j++) {
        base[4 * j] += bflo(y[j].x); base[4 * j + 1] += bfhi(y[j].x); base[4 * j + 2] += bflo(y[j].y); base[4 * j + 3] += bfhi(y[j].y);
      }
      cA = __builtin_amdgcn_readfirstlane(pa1);
      const bool pe = ((0x80u << (8 * rw)) & ~(cA - 0x0f0f0f0fu)) == 0;
      if (!pe) { PIN16(base); wait_prog(aprog, 15, cA, 0x80u << (8 * rw)); }
      const v4u p[2] = {lds_ld<v4u>(pa), lds_ld<v4u>(pa + 1024)};
      PIN16(base);
      v2u pk[4];
#pragma unroll
      for (int j = 0; j < 2; j++)
#pragma unroll
        for (int e = 0; e < 4; e++)
          pk[2 * j + (e >> 1)][e & 1] = pkbf(base[8 * j + 2 * e] + rstd * bflo(p[j][e]), base[8 * j + 2 * e + 1] + rstd * bfhi(p[j][e]));
#pragma unroll
      for (int j = 0; j < 4; j += 2) {
        const auto r0 = __builtin_amdgcn_permlane32_swap(pk[j].x, pk[j + 1].x, false, false);
        const auto r1 = __builtin_amdgcn_permlane32_swap(pk[j].y, pk[j + 1].y, false, false);
        const unsigned e0 = r0[0], e1 = r1[0], e2 = r0[1], e3 = r1[1];
        *(v4u*)((char*)out + op + 16 * j) = v4u{e0, e1, e2, e3};
      }
      ROLE_END();
    }
    }
  }
}

}
#if defined(__HIP_DEVICE_COMPILE__)
#pragma clang attribute pop
#endif

extern "C" void kernel_launch(void* const* d_in, const int* in_sizes, int n_in, void* d_out, int out_size, void* d_ws,
                              size_t ws_size, hipStream_t stream) {
  (void)in_sizes; (void)n_in; (void)out_size; (void)ws_size;
  (void)hipFuncSetAttribute((const void*)fused_kernel, hipFuncAttributeMaxDynamicSharedMemorySize, LDS_BYTES);
  fused_kernel<<<256, 1024, LDS_BYTES, stream>>>((const u16*)d_in[0], (const u16*)d_in[1], (const u16*)d_in[2], (const u16*)d_in[3],
                                                (const u16*)d_in[4], (const u16*)d_in[5], (const u16*)d_in[6], (const u16*)d_in[8],
                                                (const u16*)d_in[9], (u16*)d_out, (char*)d_ws);
}
```
